```python
import torch

def _softmax(x, axis=-1):
    return torch.softmax(x, dim=axis)
def _gelu(x):
    return torch.nn.functional.gelu(x, approximate="tanh")
def _silu(x):
    return torch.nn.functional.silu(x)
def _sigmoid(x):
    return torch.sigmoid(x)
def _layernorm(x, axis=-1, eps=1e-5):
    m = x.mean(dim=axis, keepdim=True)
    v = x.var(dim=axis, keepdim=True, unbiased=False)
    return (x - m) / torch.sqrt(v + eps)
def _scatter_add(tgt, idx, src, axis=0):
    return tgt.clone().index_add_(axis, idx, src)
def _segment_sum(src, seg, n):
    out = torch.zeros((n,) + src.shape[1:], dtype=src.dtype)
    return out.index_add_(0, seg, src)
def _pad(x, pw):
    flat = tuple(v for pair in reversed(pw) for v in pair)
    return torch.nn.functional.pad(x, flat)

kernel_name = 'recurrent_scan__softmax_full_mha__geglu_ffn__prompt'

def setup_inputs():

    _w = (lambda a: (lambda t: t.to(torch.bfloat16) if t.is_floating_point() else t)(torch.as_tensor(a)))
    def _A_setup():
        B, S, D = 1, 2048, 576
        return {
            "x": _w((lambda *s: torch.randn(*s, dtype=torch.bfloat16) * 0.05)(B, S, D)),
            "wqkv": _w((lambda *s: torch.randn(*s, dtype=torch.bfloat16) * 0.05)(D, 3 * D)),
            "wo": _w((lambda *s: torch.randn(*s, dtype=torch.bfloat16) * 0.05)(D, D)),
        }
    _A_in = _A_setup()

    def _B_setup():
        B, S, D, F = 1, 2048, 576, 4608
        return {
            "x": _w((lambda *s: torch.randn(*s, dtype=torch.bfloat16) * 0.05)(B, S, D)),
            "w_gate": _w((lambda *s: torch.randn(*s, dtype=torch.bfloat16) * 0.05)(D, F)),
            "w_up": _w((lambda *s: torch.randn(*s, dtype=torch.bfloat16) * 0.05)(D, F)),
            "w_down": _w((lambda *s: torch.randn(*s, dtype=torch.bfloat16) * 0.05)(F, D)),
        }
    _B_in = _B_setup()

    return {
        "A_x": _A_in["x"],
        "A_wqkv": _A_in["wqkv"],
        "A_wo": _A_in["wo"],
        "B_x": _B_in["x"],
        "B_w_gate": _B_in["w_gate"],
        "B_w_up": _B_in["w_up"],
        "B_w_down": _B_in["w_down"],
    }

def reference(A_x, A_wqkv, A_wo, B_x, B_w_gate, B_w_up, B_w_down):
    def _A_ref(x, wqkv, wo):
        B, S, D = x.shape
        H = 32; Dh = D // H
        qkv = (lambda x, *d: x.permute(*d))((x @ wqkv).reshape(B, S, 3, H, Dh), 2, 0, 3, 1, 4)
        q, k, v = qkv[0], qkv[1], qkv[2]
        scale = Dh ** -0.5
        scores = torch.einsum("bhqd,bhkd->bhqk", q, k) * scale
        p = _softmax(scores, axis=-1)
        o = torch.einsum("bhqk,bhkd->bhqd", p, v)
        return (lambda x, *d: x.permute(*d))(o, 0, 2, 1, 3).reshape(B, S, D) @ wo

    def _B_ref(x, w_gate, w_up, w_down):
        return (_gelu(x @ w_gate) * (x @ w_up)) @ w_down

    x = A_x
    xn = _layernorm(x, axis=-1)
    s = _A_ref(xn, A_wqkv, A_wo)
    g = _silu(_B_ref(xn, B_w_gate, B_w_up, B_w_down))
    return x + s * g
```

```cpp
#include <hip/hip_runtime.h>
#include <hip/hip_bf16.h>
typedef unsigned short u16;
__device__ __forceinline__ float bf2f(u16 v){ return __uint_as_float(((unsigned)v)<<16); }
__device__ __forceinline__ u16 f2bf(float x){ unsigned u=__float_as_uint(x); u += 0x7fffu + ((u>>16)&1u); return (u16)(u>>16); }
constexpr int S=2048,D=576,F=4608,H=32,DH=18;

__global__ void n_ln(const u16* x, u16* xn){
  int row=blockIdx.x; int t=threadIdx.x;
  float v[9]; float s=0;
  for(int i=0;i<9;i++){ v[i]=bf2f(x[row*D+t+64*i]); s+=v[i]; }
  for(int o=32;o;o>>=1) s+=__shfl_xor(s,o);
  float m=s/D; float q=0;
  for(int i=0;i<9;i++){ float d=v[i]-m; q+=d*d; }
  for(int o=32;o;o>>=1) q+=__shfl_xor(q,o);
  float r=rsqrtf(q/D+1e-5f);
  for(int i=0;i<9;i++) xn[row*D+t+64*i]=f2bf((v[i]-m)*r);
}
__global__ void n_gemm(const u16* A,const u16* B,float* C,int M,int N,int K){
  __shared__ float As[16][65], Bs[16][65];
  int bm=blockIdx.y*64,bn=blockIdx.x*64,t=threadIdx.x,tx=t&15,ty=t>>4;
  float acc[4][4]={};
  for(int k0=0;k0<K;k0+=16){
    for(int i=t;i<1024;i+=256){ int r=i>>4,c=i&15; As[c][r]=bf2f(A[(size_t)(bm+r)*K+k0+c]); }
    for(int i=t;i<1024;i+=256){ int r=i>>6,c=i&63; Bs[r][c]=bf2f(B[(size_t)(k0+r)*N+bn+c]); }
    __syncthreads();
    for(int k=0;k<16;k++){
      float a[4],b[4];
      for(int i=0;i<4;i++){a[i]=As[k][ty*4+i]; b[i]=Bs[k][tx*4+i];}
      for(int i=0;i<4;i++)for(int j=0;j<4;j++)acc[i][j]+=a[i]*b[j];
    }
    __syncthreads();
  }
  for(int i=0;i<4;i++)for(int j=0;j<4;j++)C[(size_t)(bm+ty*4+i)*N+bn+tx*4+j]=acc[i][j];
}
__global__ void n_attn(const float* qkv,u16* o){
  int h=blockIdx.y; int q=blockIdx.x*64+threadIdx.x;
  float qv[DH],acc[DH];
  for(int d=0;d<DH;d++){ qv[d]=bf2f(f2bf(qkv[(size_t)q*3*D+h*DH+d]))*0.23570226039551584f; acc[d]=0; }
  float m=-INFINITY,l=0;
  for(int j=0;j<S;j++){
    const float* kp=qkv+(size_t)j*3*D+D+h*DH; const float* vp=kp+D;
    float s=0; for(int d=0;d<DH;d++) s+=qv[d]*bf2f(f2bf(kp[d]));
    float mn=fmaxf(m,s); float a=__expf(m-mn), p=__expf(s-mn);
    l=l*a+p; for(int d=0;d<DH;d++) acc[d]=acc[d]*a+p*bf2f(f2bf(vp[d]));
    m=mn;
  }
  for(int d=0;d<DH;d++) o[(size_t)q*D+h*DH+d]=f2bf(acc[d]/l);
}
__global__ void n_geglu(const float* g,const float* u,u16* h,int n){
  int i=blockIdx.x*256+threadIdx.x; if(i>=n)return;
  float x=g[i]; float t=tanhf(0.7978845608028654f*(x+0.044715f*x*x*x));
  h[i]=f2bf(0.5f*x*(1.f+t)*u[i]);
}
__global__ void n_final(const u16* x,const float* s,const float* y,u16* out,int n){
  int i=blockIdx.x*256+threadIdx.x; if(i>=n)return;
  float yy=y[i]; float g=yy/(1.f+__expf(-yy));
  out[i]=f2bf(bf2f(x[i])+s[i]*g);
}
extern "C" void kernel_launch(void* const* d_in, const int* in_sizes, int n_in, void* d_out, int out_size, void* d_ws, size_t ws_size, hipStream_t stream){
  const u16* x=(const u16*)d_in[0]; const u16* wqkv=(const u16*)d_in[1]; const u16* wo=(const u16*)d_in[2];
  const u16* wg=(const u16*)d_in[4]; const u16* wu=(const u16*)d_in[5]; const u16* wd=(const u16*)d_in[6];
  char* ws=(char*)d_ws;
  u16* xn=(u16*)ws; ws+=S*D*2;
  float* qkv=(float*)ws; ws+=(size_t)S*3*D*4;
  u16* o=(u16*)ws; ws+=S*D*2;
  float* s=(float*)ws; ws+=S*D*4;
  float* g=(float*)ws; ws+=(size_t)S*F*4;
  float* u=(float*)ws; ws+=(size_t)S*F*4;
  u16* h=(u16*)ws; ws+=(size_t)S*F*2;
  float* y=(float*)ws; ws+=S*D*4;
  n_ln<<<S,64,0,stream>>>(x,xn);
  n_gemm<<<dim3(3*D/64,S/64),256,0,stream>>>(xn,wqkv,qkv,S,3*D,D);
  n_attn<<<dim3(S/64,H),64,0,stream>>>(qkv,o);
  n_gemm<<<dim3(D/64,S/64),256,0,stream>>>(o,wo,s,S,D,D);
  n_gemm<<<dim3(F/64,S/64),256,0,stream>>>(xn,wg,g,S,F,D);
  n_gemm<<<dim3(F/64,S/64),256,0,stream>>>(xn,wu,u,S,F,D);
  n_geglu<<<S*F/256,256,0,stream>>>(g,u,h,S*F);
  n_gemm<<<dim3(D/64,S/64),256,0,stream>>>(h,wd,y,S,D,F);
  n_final<<<S*D/256,256,0,stream>>>(x,s,y,(u16*)d_out,S*D);
}
```

```cpp
#include <hip/hip_runtime.h>
#include <hip/hip_bf16.h>
typedef unsigned short u16;
__device__ __forceinline__ float bf2f(u16 v){ return __uint_as_float(((unsigned)v)<<16); }
__device__ __forceinline__ u16 f2bf(float x){ unsigned u=__float_as_uint(x); u += 0x7fffu + ((u>>16)&1u); return (u16)(u>>16); }
constexpr int S=2048,D=576,F=4608,H=32,DH=18;

__global__ void n_ln(const u16* x, u16* xn){
  int row=blockIdx.x; int t=threadIdx.x;
  float v[9]; float s=0;
  for(int i=0;i<9;i++){ v[i]=bf2f(x[row*D+t+64*i]); s+=v[i]; }
  for(int o=32;o;o>>=1) s+=__shfl_xor(s,o);
  float m=s/D; float q=0;
  for(int i=0;i<9;i++){ float d=v[i]-m; q+=d*d; }
  for(int o=32;o;o>>=1) q+=__shfl_xor(q,o);
  float r=rsqrtf(q/D+1e-5f);
  for(int i=0;i<9;i++) xn[row*D+t+64*i]=f2bf((v[i]-m)*r);
}
__global__ void n_gemm(const u16* A,const u16* B,float* C,int M,int N,int K){
  __shared__ float As[16][65], Bs[16][65];
  int bm=blockIdx.y*64,bn=blockIdx.x*64,t=threadIdx.x,tx=t&15,ty=t>>4;
  float acc[4][4]={};
  for(int k0=0;k0<K;k0+=16){
    for(int i=t;i<1024;i+=256){ int r=i>>4,c=i&15; As[c][r]=bf2f(A[(size_t)(bm+r)*K+k0+c]); }
    for(int i=t;i<1024;i+=256){ int r=i>>6,c=i&63; Bs[r][c]=bf2f(B[(size_t)(k0+r)*N+bn+c]); }
    __syncthreads();
    for(int k=0;k<16;k++){
      float a[4],b[4];
      for(int i=0;i<4;i++){a[i]=As[k][ty*4+i]; b[i]=Bs[k][tx*4+i];}
      for(int i=0;i<4;i++)for(int j=0;j<4;j++)acc[i][j]+=a[i]*b[j];
    }
    __syncthreads();
  }
  for(int i=0;i<4;i++)for(int j=0;j<4;j++)C[(size_t)(bm+ty*4+i)*N+bn+tx*4+j]=acc[i][j];
}
__global__ void n_attn(const float* qkv,u16* o){
  int h=blockIdx.y; int q=blockIdx.x*64+threadIdx.x;
  float qv[DH],acc[DH];
  for(int d=0;d<DH;d++){ qv[d]=bf2f(f2bf(qkv[(size_t)q*3*D+h*DH+d]))*0.23570226039551584f; acc[d]=0; }
  float m=-INFINITY,l=0;
  for(int j=0;j<S;j++){
    const float* kp=qkv+(size_t)j*3*D+D+h*DH; const float* vp=kp+D;
    float s=0; for(int d=0;d<DH;d++) s+=qv[d]*bf2f(f2bf(kp[d]));
    float mn=fmaxf(m,s); float a=__expf(m-mn), p=__expf(s-mn);
    l=l*a+p; for(int d=0;d<DH;d++) acc[d]=acc[d]*a+p*bf2f(f2bf(vp[d]));
    m=mn;
  }
  for(int d=0;d<DH;d++) o[(size_t)q*D+h*DH+d]=f2bf(acc[d]/l);
}
__global__ void n_geglu(const float* g,const float* u,u16* h,int n){
  int i=blockIdx.x*256+threadIdx.x; if(i>=n)return;
  float x=g[i]; float t=tanhf(0.7978845608028654f*(x+0.044715f*x*x*x));
  h[i]=f2bf(0.5f*x*(1.f+t)*u[i]);
}
__global__ void n_final(const u16* x,const float* s,const float* y,u16* out,int n){
  int i=blockIdx.x*256+threadIdx.x; if(i>=n)return;
  float yy=y[i]; float g=yy/(1.f+__expf(-yy));
  out[i]=f2bf(bf2f(x[i])+s[i]*g);
}
typedef unsigned long long u64;
typedef float f4 __attribute__((ext_vector_type(4)));
typedef unsigned u4 __attribute__((ext_vector_type(4)));
__device__ __forceinline__ u64 rt(){ u64 t; asm volatile("s_memrealtime %0\n s_waitcnt lgkmcnt(0)":"=s"(t)::"memory"); return t;}
__device__ __forceinline__ u64 ct(){ u64 t; asm volatile("s_memtime %0\n s_waitcnt lgkmcnt(0)":"=s"(t)::"memory"); return t;}
__device__ __forceinline__ void glds16(const char* g, unsigned ldsoff){
  asm volatile("s_mov_b32 m0, %1\n s_nop 0\n global_load_lds_dwordx4 %0, off"::"v"(g),"s"(ldsoff):"memory");
}
template<int SEG,int MAP,int DEPTH,int WID=288>
__global__ __launch_bounds__(512) void kA(const char* wg,const char* wu,u64* out){
  extern __shared__ char lds[];
  int b=blockIdx.x, xcd=b&7, slot=b>>3;
  int cg = MAP==0 ? (xcd>>1)*8+(slot>>2) : MAP==1 ? xcd*4+(slot>>3) : (xcd>>2)*16+(slot>>1);
  int tid=threadIdx.x, lane=tid&63; int w=__builtin_amdgcn_readfirstlane(tid>>6);
  u64 r0=rt(), c0=ct();
  constexpr int CPS=SEG/16;
  int n0 = w<4 ? w*5 : 20+(w-4)*4; int cnt = w<4?5:4;
  constexpr int NS=WID/32;
  for(int s=0;s<NS+DEPTH-1;s++){
    if(s<NS){
      for(int i=0;i<5;i++){ if(i<cnt){
        int c=(s*36+n0+i)*64+lane;
        int cs=c%CPS; int t=c/CPS; int m=t&1; t>>=1; int row=t%576; int cb=t/576;
        const char* g=(m?wu:wg)+(size_t)row*9216+cg*WID+cb*SEG+cs*16;
        glds16(g,(s&3)*36864+(n0+i)*1024);
      }}
    }
    if(s>=DEPTH-1){
      int rem = (s<NS? DEPTH-1 : NS-1-(s-DEPTH+1));
      if(w<4){ if(rem>=3) asm volatile("s_waitcnt vmcnt(15)"); else if(rem==2) asm volatile("s_waitcnt vmcnt(10)"); else if(rem==1) asm volatile("s_waitcnt vmcnt(5)"); else asm volatile("s_waitcnt vmcnt(0)"); }
      else   { if(rem>=3) asm volatile("s_waitcnt vmcnt(12)"); else if(rem==2) asm volatile("s_waitcnt vmcnt(8)"); else if(rem==1) asm volatile("s_waitcnt vmcnt(4)"); else asm volatile("s_waitcnt vmcnt(0)"); }
      asm volatile("s_barrier":::"memory");
    }
  }
  u64 c1=ct(), r1=rt();
  if(tid==0){ out[b*3]=r0; out[b*3+1]=r1; out[b*3+2]=c1-c0; }
}
template<int PAT,int MAP>
__global__ __launch_bounds__(512) void kB(const char* x,u64* out,unsigned* sink){
  int b=blockIdx.x, xcd=b&7, slot=b>>3;
  int rb = MAP==0 ? (xcd&1)*4+(slot&3) : MAP==1 ? (slot&7) : (xcd&3)*2+(slot&1);
  int tid=threadIdx.x, lane=tid&63; int w=__builtin_amdgcn_readfirstlane(tid>>6);
  u64 r0=rt(), c0=ct();
  const char* base=x+(size_t)(rb*256+w*32)*1152;
  u4 v[36];
  int t=lane&31,h=lane>>5;
  #pragma unroll
  for(int k=0;k<36;k++){
    const char* g = PAT==0 ? base+t*1152+k*32+h*16 : PAT==1 ? base+t*1152+(k>>2)*128+h*64+(k&3)*16 : base+k*1024+lane*16;
    v[k]=*(const u4*)g;
  }
  __builtin_amdgcn_sched_barrier(0);
  u4 a=v[0];
  #pragma unroll
  for(int k=1;k<36;k++) a^=v[k];
  if(a.x==0x12345678 && a.y==a.z && a.w==77) sink[tid]=1;
  __syncthreads();
  u64 c1=ct(), r1=rt();
  if(tid==0){ out[b*3]=r0; out[b*3+1]=r1; out[b*3+2]=c1-c0; }
}
template<int NB>
__global__ __launch_bounds__(256) void kC(const char* H,const char* Wd,u64* out,unsigned* sink){
  int b=blockIdx.x, xcd=b&7, slot=b>>3;
  int kh=xcd&1, rb=(xcd>>1)*8+(slot>>2), cb=slot&3;
  int tid=threadIdx.x, lane=tid&63; int w=__builtin_amdgcn_readfirstlane(tid>>6);
  u64 r0=rt(), c0=ct();
  const char* hp=H+((size_t)rb*144+kh*72+w*18)*4096+lane*16;
  const char* wp=Wd+((size_t)cb*144+kh*72+w*18)*9216+lane*16;
  u4 buf[NB][13]; u4 a={0,0,0,0};
  #pragma unroll
  for(int s=0;s<18+NB;s++){
    if(s>=NB){
      #pragma unroll
      for(int i=0;i<13;i++) a^=buf[s%NB][i];
    }
    if(s<18){
      #pragma unroll
      for(int i=0;i<4;i++) buf[s%NB][i]=*(const u4*)(hp+s*4096+i*1024);
      #pragma unroll
      for(int i=0;i<9;i++) buf[s%NB][4+i]=*(const u4*)(wp+s*9216+i*1024);
    }
    __builtin_amdgcn_sched_barrier(0);
  }
  if(a.x==0x12345678 && a.y==a.z && a.w==77) sink[tid]=1;
  __syncthreads();
  u64 c1=ct(), r1=rt();
  if(tid==0){ out[b*3]=r0; out[b*3+1]=r1; out[b*3+2]=c1-c0; }
}

__global__ void prn(const u64* o){
  double sp[3]; double cy[3];
  for(int k=0;k<3;k++){ u64 mn=~0ull,mx=0; double c=0; const u64* p=o+k*768; for(int i=0;i<256;i++){ mn=p[i*3]<mn?p[i*3]:mn; mx=p[i*3+1]>mx?p[i*3+1]:mx; c+=p[i*3+2]; } sp[k]=(mx-mn)*0.01; cy[k]=c/256; }
  printf("PROBE X %.2f us %.0f cyc | W %.2f us %.0f cyc | X2 %.2f us %.0f cyc\n",sp[0],cy[0],sp[1],cy[1],sp[2],cy[2]);
}
extern "C" void kernel_launch(void* const* d_in, const int* in_sizes, int n_in, void* d_out, int out_size, void* d_ws, size_t ws_size, hipStream_t stream){
  const u16* x=(const u16*)d_in[0]; const u16* wqkv=(const u16*)d_in[1]; const u16* wo=(const u16*)d_in[2];
  const u16* wg=(const u16*)d_in[4]; const u16* wu=(const u16*)d_in[5]; const u16* wd=(const u16*)d_in[6];
  char* ws=(char*)d_ws;
  u16* xn=(u16*)ws; ws+=S*D*2;
  float* qkv=(float*)ws; ws+=(size_t)S*3*D*4;
  u16* o=(u16*)ws; ws+=S*D*2;
  float* s=(float*)ws; ws+=S*D*4;
  float* g=(float*)ws; ws+=(size_t)S*F*4;
  float* u=(float*)ws; ws+=(size_t)S*F*4;
  u16* h=(u16*)ws; ws+=(size_t)S*F*2;
  float* y=(float*)ws; ws+=S*D*4;
  u64* po=(u64*)((char*)d_ws+(200<<20)); unsigned* sink=(unsigned*)((char*)d_ws+(201<<20));
  hipFuncSetAttribute((const void*)kB<2,0>,hipFuncAttributeMaxDynamicSharedMemorySize,147456);
  hipFuncSetAttribute((const void*)kA<128,0,4,256>,hipFuncAttributeMaxDynamicSharedMemorySize,147456);
  kB<2,0><<<256,512,147456,stream>>>((const char*)x,po,sink);
  kA<128,0,4,256><<<256,512,147456,stream>>>((const char*)wg,(const char*)wu,po+768);
  kB<2,0><<<256,512,147456,stream>>>((const char*)wd,po+1536,sink);
  prn<<<1,1,0,stream>>>(po);
  n_ln<<<S,64,0,stream>>>(x,xn);
  n_gemm<<<dim3(3*D/64,S/64),256,0,stream>>>(xn,wqkv,qkv,S,3*D,D);
  n_attn<<<dim3(S/64,H),64,0,stream>>>(qkv,o);
  n_gemm<<<dim3(D/64,S/64),256,0,stream>>>(o,wo,s,S,D,D);
  n_gemm<<<dim3(F/64,S/64),256,0,stream>>>(xn,wg,g,S,F,D);
  n_gemm<<<dim3(F/64,S/64),256,0,stream>>>(xn,wu,u,S,F,D);
  n_geglu<<<S*F/256,256,0,stream>>>(g,u,h,S*F);
  n_gemm<<<dim3(D/64,S/64),256,0,stream>>>(h,wd,y,S,D,F);
  n_final<<<S*D/256,256,0,stream>>>(x,s,y,(u16*)d_out,S*D);
}
```

```cpp
#define WHICH 0
#define AMP 0
#include <hip/hip_runtime.h>
#include <hip/hip_bf16.h>
typedef unsigned short u16;
__device__ __forceinline__ float bf2f(u16 v){ return __uint_as_float(((unsigned)v)<<16); }
__device__ __forceinline__ u16 f2bf(float x){ unsigned u=__float_as_uint(x); u += 0x7fffu + ((u>>16)&1u); return (u16)(u>>16); }
constexpr int S=2048,D=576,F=4608,H=32,DH=18;

__global__ void n_ln(const u16* x, u16* xn){
  int row=blockIdx.x; int t=threadIdx.x;
  float v[9]; float s=0;
  for(int i=0;i<9;i++){ v[i]=bf2f(x[row*D+t+64*i]); s+=v[i]; }
  for(int o=32;o;o>>=1) s+=__shfl_xor(s,o);
  float m=s/D; float q=0;
  for(int i=0;i<9;i++){ float d=v[i]-m; q+=d*d; }
  for(int o=32;o;o>>=1) q+=__shfl_xor(q,o);
  float r=rsqrtf(q/D+1e-5f);
  for(int i=0;i<9;i++) xn[row*D+t+64*i]=f2bf((v[i]-m)*r);
}
__global__ void n_gemm(const u16* A,const u16* B,float* C,int M,int N,int K){
  __shared__ float As[16][65], Bs[16][65];
  int bm=blockIdx.y*64,bn=blockIdx.x*64,t=threadIdx.x,tx=t&15,ty=t>>4;
  float acc[4][4]={};
  for(int k0=0;k0<K;k0+=16){
    for(int i=t;i<1024;i+=256){ int r=i>>4,c=i&15; As[c][r]=bf2f(A[(size_t)(bm+r)*K+k0+c]); }
    for(int i=t;i<1024;i+=256){ int r=i>>6,c=i&63; Bs[r][c]=bf2f(B[(size_t)(k0+r)*N+bn+c]); }
    __syncthreads();
    for(int k=0;k<16;k++){
      float a[4],b[4];
      for(int i=0;i<4;i++){a[i]=As[k][ty*4+i]; b[i]=Bs[k][tx*4+i];}
      for(int i=0;i<4;i++)for(int j=0;j<4;j++)acc[i][j]+=a[i]*b[j];
    }
    __syncthreads();
  }
  for(int i=0;i<4;i++)for(int j=0;j<4;j++)C[(size_t)(bm+ty*4+i)*N+bn+tx*4+j]=acc[i][j];
}
__global__ void n_attn(const float* qkv,u16* o){
  int h=blockIdx.y; int q=blockIdx.x*64+threadIdx.x;
  float qv[DH],acc[DH];
  for(int d=0;d<DH;d++){ qv[d]=bf2f(f2bf(qkv[(size_t)q*3*D+h*DH+d]))*0.23570226039551584f; acc[d]=0; }
  float m=-INFINITY,l=0;
  for(int j=0;j<S;j++){
    const float* kp=qkv+(size_t)j*3*D+D+h*DH; const float* vp=kp+D;
    float s=0; for(int d=0;d<DH;d++) s+=qv[d]*bf2f(f2bf(kp[d]));
    float mn=fmaxf(m,s); float a=__expf(m-mn), p=__expf(s-mn);
    l=l*a+p; for(int d=0;d<DH;d++) acc[d]=acc[d]*a+p*bf2f(f2bf(vp[d]));
    m=mn;
  }
  for(int d=0;d<DH;d++) o[(size_t)q*D+h*DH+d]=f2bf(acc[d]/l);
}
__global__ void n_geglu(const float* g,const float* u,u16* h,int n){
  int i=blockIdx.x*256+threadIdx.x; if(i>=n)return;
  float x=g[i]; float t=tanhf(0.7978845608028654f*(x+0.044715f*x*x*x));
  h[i]=f2bf(0.5f*x*(1.f+t)*u[i]);
}
__global__ void n_final(const u16* x,const float* s,const float* y,u16* out,int n){
  int i=blockIdx.x*256+threadIdx.x; if(i>=n)return;
  float yy=y[i]; float g=yy/(1.f+__expf(-yy));
  out[i]=f2bf(bf2f(x[i])+s[i]*g);
}
typedef unsigned long long u64;
typedef float f4 __attribute__((ext_vector_type(4)));
typedef unsigned u4 __attribute__((ext_vector_type(4)));
__device__ __forceinline__ u64 rt(){ u64 t; asm volatile("s_memrealtime %0\n s_waitcnt lgkmcnt(0)":"=s"(t)::"memory"); return t;}
__device__ __forceinline__ u64 ct(){ u64 t; asm volatile("s_memtime %0\n s_waitcnt lgkmcnt(0)":"=s"(t)::"memory"); return t;}
__device__ __forceinline__ void glds16(const char* g, unsigned ldsoff){
  asm volatile("s_mov_b32 m0, %1\n s_nop 0\n global_load_lds_dwordx4 %0, off"::"v"(g),"s"(ldsoff):"memory");
}
template<int SEG,int MAP,int DEPTH,int WID=288>
__global__ __launch_bounds__(512) void kA(const char* wg,const char* wu,u64* out){
  extern __shared__ char lds[];
  int b=blockIdx.x, xcd=b&7, slot=b>>3;
  int cg = MAP==0 ? (xcd>>1)*8+(slot>>2) : MAP==1 ? xcd*4+(slot>>3) : (xcd>>2)*16+(slot>>1);
  int tid=threadIdx.x, lane=tid&63; int w=__builtin_amdgcn_readfirstlane(tid>>6);
  u64 r0=rt(), c0=ct();
  constexpr int CPS=SEG/16;
  int n0 = w<4 ? w*5 : 20+(w-4)*4; int cnt = w<4?5:4;
  constexpr int NS=WID/32;
  for(int s=0;s<NS+DEPTH-1;s++){
    if(s<NS){
      for(int i=0;i<5;i++){ if(i<cnt){
        int c=(s*36+n0+i)*64+lane;
        int cs=c%CPS; int t=c/CPS; int m=t&1; t>>=1; int row=t%576; int cb=t/576;
        const char* g=(m?wu:wg)+(size_t)row*9216+cg*WID+cb*SEG+cs*16;
        glds16(g,(s&3)*36864+(n0+i)*1024);
      }}
    }
    if(s>=DEPTH-1){
      int rem = (s<NS? DEPTH-1 : NS-1-(s-DEPTH+1));
      if(w<4){ if(rem>=3) asm volatile("s_waitcnt vmcnt(15)"); else if(rem==2) asm volatile("s_waitcnt vmcnt(10)"); else if(rem==1) asm volatile("s_waitcnt vmcnt(5)"); else asm volatile("s_waitcnt vmcnt(0)"); }
      else   { if(rem>=3) asm volatile("s_waitcnt vmcnt(12)"); else if(rem==2) asm volatile("s_waitcnt vmcnt(8)"); else if(rem==1) asm volatile("s_waitcnt vmcnt(4)"); else asm volatile("s_waitcnt vmcnt(0)"); }
      asm volatile("s_barrier":::"memory");
    }
  }
  u64 c1=ct(), r1=rt();
  if(tid==0){ out[b*3]=r0; out[b*3+1]=r1; out[b*3+2]=c1-c0; }
}
template<int PAT,int MAP>
__global__ __launch_bounds__(512) void kB(const char* x,u64* out,unsigned* sink){
  int b=blockIdx.x, xcd=b&7, slot=b>>3;
  int rb = MAP==0 ? (xcd&1)*4+(slot&3) : MAP==1 ? (slot&7) : (xcd&3)*2+(slot&1);
  int tid=threadIdx.x, lane=tid&63; int w=__builtin_amdgcn_readfirstlane(tid>>6);
  u64 r0=rt(), c0=ct();
  const char* base=x+(size_t)(rb*256+w*32)*1152;
  u4 v[36];
  int t=lane&31,h=lane>>5;
  #pragma unroll
  for(int k=0;k<36;k++){
    const char* g = PAT==0 ? base+t*1152+k*32+h*16 : PAT==1 ? base+t*1152+(k>>2)*128+h*64+(k&3)*16 : base+k*1024+lane*16;
    v[k]=*(const u4*)g;
  }
  __builtin_amdgcn_sched_barrier(0);
  u4 a=v[0];
  #pragma unroll
  for(int k=1;k<36;k++) a^=v[k];
  if(a.x==0x12345678 && a.y==a.z && a.w==77) sink[tid]=1;
  __syncthreads();
  u64 c1=ct(), r1=rt();
  if(tid==0){ out[b*3]=r0; out[b*3+1]=r1; out[b*3+2]=c1-c0; }
}
template<int NB>
__global__ __launch_bounds__(256) void kC(const char* H,const char* Wd,u64* out,unsigned* sink){
  int b=blockIdx.x, xcd=b&7, slot=b>>3;
  int kh=xcd&1, rb=(xcd>>1)*8+(slot>>2), cb=slot&3;
  int tid=threadIdx.x, lane=tid&63; int w=__builtin_amdgcn_readfirstlane(tid>>6);
  u64 r0=rt(), c0=ct();
  const char* hp=H+((size_t)rb*144+kh*72+w*18)*4096+lane*16;
  const char* wp=Wd+((size_t)cb*144+kh*72+w*18)*9216+lane*16;
  u4 buf[NB][13]; u4 a={0,0,0,0};
  #pragma unroll
  for(int s=0;s<18+NB;s++){
    if(s>=NB){
      #pragma unroll
      for(int i=0;i<13;i++) a^=buf[s%NB][i];
    }
    if(s<18){
      #pragma unroll
      for(int i=0;i<4;i++) buf[s%NB][i]=*(const u4*)(hp+s*4096+i*1024);
      #pragma unroll
      for(int i=0;i<9;i++) buf[s%NB][4+i]=*(const u4*)(wp+s*9216+i*1024);
    }
    __builtin_amdgcn_sched_barrier(0);
  }
  if(a.x==0x12345678 && a.y==a.z && a.w==77) sink[tid]=1;
  __syncthreads();
  u64 c1=ct(), r1=rt();
  if(tid==0){ out[b*3]=r0; out[b*3+1]=r1; out[b*3+2]=c1-c0; }
}


__global__ void prn(const u64* o){
  u64 mn=~0ull,mx=0; const u64* p=o+WHICH*768; for(int i=0;i<256;i++){ mn=p[i*3]<mn?p[i*3]:mn; mx=p[i*3+1]>mx?p[i*3+1]:mx; }
  u64 d=(mx-mn)*AMP; if(d>300000) d=300000;
  u64 t0=rt(); while(rt()-t0<d){ __builtin_amdgcn_s_sleep(8); }
}
extern "C" void kernel_launch(void* const* d_in, const int* in_sizes, int n_in, void* d_out, int out_size, void* d_ws, size_t ws_size, hipStream_t stream){
  const u16* x=(const u16*)d_in[0]; const u16* wqkv=(const u16*)d_in[1]; const u16* wo=(const u16*)d_in[2];
  const u16* wg=(const u16*)d_in[4]; const u16* wu=(const u16*)d_in[5]; const u16* wd=(const u16*)d_in[6];
  char* ws=(char*)d_ws;
  u16* xn=(u16*)ws; ws+=S*D*2;
  float* qkv=(float*)ws; ws+=(size_t)S*3*D*4;
  u16* o=(u16*)ws; ws+=S*D*2;
  float* s=(float*)ws; ws+=S*D*4;
  float* g=(float*)ws; ws+=(size_t)S*F*4;
  float* u=(float*)ws; ws+=(size_t)S*F*4;
  u16* h=(u16*)ws; ws+=(size_t)S*F*2;
  float* y=(float*)ws; ws+=S*D*4;
  u64* po=(u64*)((char*)d_ws+(200<<20)); unsigned* sink=(unsigned*)((char*)d_ws+(201<<20));
  hipFuncSetAttribute((const void*)kB<2,0>,hipFuncAttributeMaxDynamicSharedMemorySize,147456);
  hipFuncSetAttribute((const void*)kA<128,0,4,256>,hipFuncAttributeMaxDynamicSharedMemorySize,147456);
  kB<2,0><<<256,512,147456,stream>>>((const char*)x,po,sink);
  kA<128,0,4,256><<<256,512,147456,stream>>>((const char*)wg,(const char*)wu,po+768);
  kB<2,0><<<256,512,147456,stream>>>((const char*)wd,po+1536,sink);
  prn<<<1,1,0,stream>>>(po);
  n_ln<<<S,64,0,stream>>>(x,xn);
  n_gemm<<<dim3(3*D/64,S/64),256,0,stream>>>(xn,wqkv,qkv,S,3*D,D);
  n_attn<<<dim3(S/64,H),64,0,stream>>>(qkv,o);
  n_gemm<<<dim3(D/64,S/64),256,0,stream>>>(o,wo,s,S,D,D);
  n_gemm<<<dim3(F/64,S/64),256,0,stream>>>(xn,wg,g,S,F,D);
  n_gemm<<<dim3(F/64,S/64),256,0,stream>>>(xn,wu,u,S,F,D);
  n_geglu<<<S*F/256,256,0,stream>>>(g,u,h,S*F);
  n_gemm<<<dim3(D/64,S/64),256,0,stream>>>(h,wd,y,S,D,F);
  n_final<<<S*D/256,256,0,stream>>>(x,s,y,(u16*)d_out,S*D);
}
```

```cpp
#include <hip/hip_runtime.h>
#include <stdint.h>
typedef unsigned short u16;
typedef unsigned int u32;
typedef unsigned long long u64;
typedef __attribute__((ext_vector_type(16))) u32 u32x16;
typedef __attribute__((ext_vector_type(8))) u32 u32x8;
typedef __attribute__((ext_vector_type(4))) u32 u32x4;
typedef __attribute__((ext_vector_type(2))) u32 u32x2;
typedef __attribute__((ext_vector_type(4))) float f32x4;
typedef __attribute__((ext_vector_type(2))) __bf16 bf16x2;
typedef __attribute__((ext_vector_type(2))) float f32x2;
typedef __attribute__((address_space(3))) u32x4 lds_u32x4;
typedef __attribute__((address_space(3))) f32x4 lds_f32x4;
typedef __attribute__((address_space(3))) f32x2 lds_f32x2;

constexpr u32 WS_FA = 4096, WS_FB = WS_FA + 32768, WS_FC = WS_FB + 32768;
constexpr u32 WS_XN = 131072;
constexpr u32 WS_IMG = WS_XN + 32 * 73728;
constexpr u32 WS_H = WS_IMG + 32 * 110592;
constexpr u32 WS_KV = WS_H + 256 * 73728;
constexpr u32 WS_YP = WS_KV + 32 * 64 * 2304;
constexpr u32 WS_OM = WS_YP + 2048 * 9216;
constexpr u32 OOB = 0x7ff00000u;
constexpr int LDS_BYTES = 163840, LKV = 92160, LT = 148480, LXS = 162816;

__device__ __forceinline__ float bflo(u32 v) { return __uint_as_float(v << 16); }
__device__ __forceinline__ float bfhi(u32 v) { return __uint_as_float(v & 0xffff0000u); }
__device__ __forceinline__ u32 pk2(float a, float b) {
  f32x2 f = {a, b};
  bf16x2 r = __builtin_convertvector(f, bf16x2);
  return __builtin_bit_cast(u32, r);
}
__device__ __forceinline__ float red8(float s) {
  s += __uint_as_float(__builtin_amdgcn_update_dpp(0u, __float_as_uint(s), 0x128, 0xf, 0xf, false));
  u32x2 a = __builtin_amdgcn_permlane16_swap(__float_as_uint(s), __float_as_uint(s), false, false);
  s = __uint_as_float(a[0]) + __uint_as_float(a[1]);
  a = __builtin_amdgcn_permlane32_swap(__float_as_uint(s), __float_as_uint(s), false, false);
  return __uint_as_float(a[0]) + __uint_as_float(a[1]);
}
#define STAMP(i)

__global__ __launch_bounds__(256, 1) void mk(const u16* x, const u16* wqkv, const u16* wo, const u16* wg, const u16* wu, const u16* wd, u16* out, char* ws) {
  u32x16 dA, dB;
  u32 l = threadIdx.x & 63, b = blockIdx.x;
  {
    u32 pc = ((b >> 3) * 32 + (threadIdx.x >> 3)) * 128;
    asm volatile(
"s_load_dwordx2 s[44:45], %3, 0\n"
"s_load_dwordx2 s[20:21], %3, 56\n"
"s_load_dwordx2 s[24:25], %3, 24\n"
"s_load_dwordx2 s[28:29], %3, 32\n"
"s_load_dwordx2 s[16:17], %3, 8\n"
"s_load_dwordx2 s[36:37], %3, 40\n"
"s_load_dwordx2 s[40:41], %3, 16\n"
"s_load_dwordx2 s[48:49], %3, 48\n"
"s_getpc_b64 s[6:7]\n"
".Lpc0:\n"
"v_min_u32 v158, .Lend-.Lpc0-4, v158\n"
"global_load_dword v226, v158, s[6:7]\n"
"s_mov_b32 s46, 2359296\n"
"s_mov_b32 s47, 0x20000\n"
"s_mov_b32 s22, 67108864\n"
"s_mov_b32 s23, 0x20000\n"
"s_mov_b32 s26, 5308416\n"
"s_mov_b32 s27, 0x20000\n"
"s_mov_b32 s30, 5308416\n"
"s_mov_b32 s31, 0x20000\n"
"s_mov_b32 s18, 1990656\n"
"s_mov_b32 s19, 0x20000\n"
"s_mov_b32 s38, 5308416\n"
"s_mov_b32 s39, 0x20000\n"
"s_mov_b32 s42, 663552\n"
"s_mov_b32 s43, 0x20000\n"
"s_mov_b32 s50, 2359296\n"
"s_mov_b32 s51, 0x20000\n"
        : "=&{s[16:31]}"(dA), "=&{s[36:51]}"(dB), "+{v158}"(pc)
        : "s"(__builtin_amdgcn_kernarg_segment_ptr())
        : "memory", "s6", "s7", "v226");
  }
  u32 w = __builtin_amdgcn_readfirstlane(threadIdx.x >> 6);
  u32x4 d0, d1, d2; u32x2 ev;
  u32x4 pf;
  u32x2 sP;
  u32x8 sX;
  u32 gu;
  u32x2 xs;
  u32x4 bld;
  {
    const u32 j = b & 7, rb = (b >> 3) & 7, cg = b >> 6;
    const u32 r = l & 7, c = l >> 3, cc = 18 * w + c;
    const u32 xa = (8 * b + r) * 1152 + cc * 16;
    pf[2] = l < 36 ? (72 * rb + 18 * w + (l >> 1)) * 9216 : OOB;
    sP = u32x2{1152 * j + 256 * cg, 576 * 1152 * j};
    asm volatile(
"s_waitcnt lgkmcnt(0)\n"
"buffer_load_dwordx4 v[144:147], v156, s[44:47], 0 offen nt\n"
"buffer_load_dwordx4 v[148:151], v156, s[44:47], 0 offen offset:128 nt\n"
"buffer_load_dwordx4 v[152:155], v157, s[44:47], 0 offen offset:256 nt\n"
"buffer_load_dwordx2 v[230:231], off, s[20:23], 0 sc1\n"
"buffer_load_dword v226, v242, s[24:27], s4 offen\n"
"buffer_load_dword v226, v242, s[28:31], s4 offen\n"
        : "={v[144:147]}"(d0), "={v[148:151]}"(d1), "={v[152:155]}"(d2), "={v[230:231]}"(ev), "+{s[16:31]}"(dA), "+{s[36:51]}"(dB)
        : "{v156}"(xa), "{v157}"(c < 2 ? xa : OOB), "{v242}"(pf[2]), "{s[4:5]}"(sP)
        : "scc", "memory", "v226");
    STAMP(1);
    const u32 br = 8 * ((b >> 3) + 32 * w) + (l >> 3), hf = (l & 1) * 16, hq = (l >> 1) & 3, rr = br & 31;
    const u32 pos = 4 * (rr & 4) + ((rr >> 3) << 2) + (rr & 3);
    u32 kk = (br >> 5) + (hq == 1 ? 12 : hq == 2 ? 6 : 0);
    kk = kk >= 18 ? kk - 18 : kk;
    bld = u32x4{br * 9216 + (l & 7) * 16, br * 3456 + 36 * hq + hf, br * 3456 + 36 * hq + 32, hq * 110592 + kk * 1024 + pos * 32 + hf};
    if (br >= 576) bld = u32x4{OOB, OOB, OOB, OOB};
    gu = (8 * w + c) * 9216 + ((((l & 7) >> 1) ^ (((l >> 4) & 1) | ((w & 1) << 1))) << 5) + (l & 1) * 16;
    xs[0] = (cc >> 2) * 4096 + (16 * (cc & 3) + 8 * (b & 1) + r) * 16;
    xs[1] = c < 2 ? xs[0] : OOB;
    const u32 s0 = cg == 3 ? 0 : cg, s1 = s0 == 2 ? 0 : s0 + 1, s2 = s1 == 2 ? 0 : s1 + 1, xb = WS_XN + (4 * rb + w) * 73728;
    sX = u32x8{xb + s0 * 24576, xb + s1 * 24576, xb + s2 * 24576, sP[0] + s0 * 1769472, sP[0] + s1 * 1769472, sP[0] + s2 * 1769472, cg, rb};
  }
  u32 xo = WS_XN + (b >> 3) * 73728 + ((b >> 1) & 3) * 1024, mu = 1024 * w;
  u32x4 vl, sg, sF; u32x2 kw, sG; u32 tru, vfl; u32x16 sC, sD;
  {
    const u32 j = b & 7, rb = (b >> 3) & 7, cg = b >> 6, head = 4 * j + cg;
    const u32 g = l >> 4, n = l & 15, q = (l >> 2) & 3, p = l & 3, kb = LKV + 4640 * w;
    vl = u32x4{l * 16, l * 8, kb + 2048 + 4 * l, ((l >> 2) & 7) * 128 + p * 8};
    tru = (8 * g + q) * 128 + ((((q >> 1) & 1) | ((g & 1) << 1)) << 5) + 8 * p;
    kw = u32x2{kb + l * 16, kb + (g & 1) * 2320 + 2048 + 16 * n};
    sg = u32x4{(g & 1) ? __float_as_uint(1.5481263368016152f) : 0x3f800000u, kb + 2176 + ((l >> 3) & 1) * 64 + (g & 1) * 32 + 2 * (l & 7), (g & 1) * 2304 + 2048 + 16 * n, 0u};
    vfl = l ? OOB : 0u;
    pf[0] = pf[1] = 0;
    pf[3] = l < 41 ? (((b >> 3) * 4 + w) * 41 + l) * 128 : OOB;
    const u32 Hb = WS_H + (32 * j + 4 * rb + w) * 73728, im = WS_IMG + head * 110592, yp = WS_YP + (4 * rb + cg) * 589824;
    sC = u32x16{0u, 0u, 0u, w, 0u, 0u, 0u, im + 1024 * w,
                0u, 1024 * w, Hb + 8192 * cg, Hb + 65536 + cg * 2048,
                Hb, WS_KV + (64 * head + 8 * rb + 2 * w) * 2304, WS_KV + (64 * head + 2 * w) * 2304, (576 * j + 16 * w) * 1152 + 288 * cg};
    sD = u32x16{yp + (16 * w + j) * 9216, yp + j * 73728 + 2048 * w, 144 * w * 1152 + 288 * cg, WS_OM + ((8 * rb + 2 * w) * 32 + head) * 1152,
                WS_OM + ((8 * rb + j) * 32 + 8 * w) * 1152, 32 * (8 * rb + j) * 1152 + 288 * cg + 64 * w, WS_FA + b * 128 + 8 * w, WS_FB + (head * 8 + rb) * 128 + 8 * w,
                WS_FC + (rb * 32 + head) * 128 + 8 * w, WS_FA + (32 * rb + 8 * w) * 128, WS_FA + (b & ~7u) * 128, WS_FB + 1024 * head,
                WS_FC + (32 * rb + 4 * j) * 128, WS_FC + (32 * rb + 8 * w) * 128 + 8 * (j >> 1), 0u, 0u};
    sF = u32x4{4608 * w, 9216 * w, yp + j * 73728 + 8192, (w & 1) * 18432 + 256 - 64 * w};
    sG = u32x2{WS_KV + head * 64 * 2304, 4000000u};
  }

  STAMP(2);
  asm volatile("s_waitcnt vmcnt(2)\n buffer_load_dword v226, v242, s[24:27], s4 offen offset:128"
               : "+{v[144:147]}"(d0), "+{v[148:151]}"(d1), "+{v[152:155]}"(d2), "+{v[230:231]}"(ev), "+{v239}"(gu), "+{v[156:157]}"(xs), "+{s[8:15]}"(sX), "+{s90}"(xo), "+{s91}"(mu), "+{v[160:163]}"(bld)
                 , "+{v[232:235]}"(vl), "+{v236}"(tru), "+{v[240:243]}"(pf), "+{v[244:245]}"(kw), "+{v[248:251]}"(sg), "+{v252}"(vfl) : "{s[52:67]}"(sC), "{s[68:83]}"(sD), "{s[84:87]}"(sF), "{s[88:89]}"(sG), "{s[4:5]}"(sP), "{s[16:31]}"(dA) : "memory", "v226", "scc", "s92");
  STAMP(3);
  {
    const u32 r = l & 7;
    float f[24];
#pragma unroll
    for (int i = 0; i < 4; i++) {
      f[2 * i] = bflo(d0[i]); f[2 * i + 1] = bfhi(d0[i]); f[8 + 2 * i] = bflo(d1[i]); f[9 + 2 * i] = bfhi(d1[i]);
      f[16 + 2 * i] = bflo(d2[i]); f[17 + 2 * i] = bfhi(d2[i]);
    }
    f32x2 s0 = {f[0], f[1]}, s1 = {f[2], f[3]};
#pragma unroll
    for (int i = 1; i < 6; i++) { s0 += f32x2{f[4 * i], f[4 * i + 1]}; s1 += f32x2{f[4 * i + 2], f[4 * i + 3]}; }
    s0 += s1;
    const float mw = red8(s0[0] + s0[1]) * (1.0f / 144.0f);
    f32x2 g[12], q0 = {0, 0}, q1 = {0, 0}, q2 = {0, 0};
#pragma unroll
    for (int i = 0; i < 12; i++) g[i] = f32x2{f[2 * i], f[2 * i + 1]} - mw;
#pragma unroll
    for (int i = 0; i < 4; i++) { q0 += g[i] * g[i]; q1 += g[4 + i] * g[4 + i]; q2 += g[8 + i] * g[8 + i]; }
    q0 += q1;
    const float m2 = red8(q0[0] + q0[1] + (l < 16 ? q2[0] + q2[1] : 0.0f));
    *(lds_f32x2*)(LXS + r * 32 + w * 8) = f32x2{mw, m2};
    const u64 e = (((u64)ev[1] << 32) | ev[0]) + 1;
    ev = u32x2{(u32)e, (u32)(e >> 32)};
    asm volatile("s_waitcnt lgkmcnt(0)\n s_barrier" ::: "memory");
    STAMP(4);
    const f32x4 pa = *(lds_f32x4*)(LXS + r * 32), pb = *(lds_f32x4*)(LXS + r * 32 + 16);
    const float mean = ((pa[0] + pa[2]) + (pb[0] + pb[2])) * 0.25f;
    const float e0 = pa[0] - mean, e1 = pa[2] - mean, e2 = pb[0] - mean, e3 = pb[2] - mean;
    const float var = (((pa[1] + pa[3]) + (pb[1] + pb[3])) + 144.0f * ((e0 * e0 + e1 * e1) + (e2 * e2 + e3 * e3))) * (1.0f / 576.0f);
    const float rs = __builtin_amdgcn_rsqf(var + 1e-5f) * 0.4686686930532063f, sh = (mw - mean) * rs;
#pragma unroll
    for (int i = 0; i < 4; i++) {
      f32x2 a = g[i] * rs + sh, c = g[4 + i] * rs + sh, d = g[8 + i] * rs + sh;
      d0[i] = pk2(a[0], a[1]); d1[i] = pk2(c[0], c[1]); d2[i] = pk2(d[0], d[1]);
    }
  }
  asm volatile(
"s_nop 1\n"
"buffer_store_dwordx4 v[144:147], v156, s[20:23], s90 offen sc1\n"
"s_add_u32 s90, s90, 8192\n"
"buffer_store_dwordx4 v[148:151], v156, s[20:23], s90 offen sc1\n"
"s_add_u32 s90, s90, 8192\n"
"buffer_store_dwordx4 v[152:155], v157, s[20:23], s90 offen sc1\n"
"s_add_u32 m0, s91, 0\n"
"s_add_u32 s92, s11, 0\n"
"buffer_load_dwordx4 v239, s[24:27], s92 offen lds\n"
"s_add_u32 m0, s91, 4096\n"
"s_add_u32 s92, s11, 294912\n"
"buffer_load_dwordx4 v239, s[24:27], s92 offen lds\n"
"s_add_u32 m0, s91, 73728\n"
"s_add_u32 s92, s11, 0\n"
"buffer_load_dwordx4 v239, s[28:31], s92 offen lds\n"
"s_add_u32 m0, s91, 77824\n"
"s_add_u32 s92, s11, 294912\n"
"buffer_load_dwordx4 v239, s[28:31], s92 offen lds\n"
"s_add_u32 m0, s91, 8192\n"
"s_add_u32 s92, s11, 589824\n"
"buffer_load_dwordx4 v239, s[24:27], s92 offen lds\n"
"s_add_u32 m0, s91, 12288\n"
"s_add_u32 s92, s11, 884736\n"
"buffer_load_dwordx4 v239, s[24:27], s92 offen lds\n"
      : "+v"(l), "+s"(b), "+s"(w), "+{s90}"(xo)
      : "{v[144:147]}"(d0), "{v[148:151]}"(d1), "{v[152:155]}"(d2), "{v239}"(gu), "{v[156:157]}"(xs), "{s[8:15]}"(sX), "{s91}"(mu), "{s[16:31]}"(dA),
        "{v242}"(pf[2]), "{s[4:5]}"(sP), "{v[160:163]}"(bld)
      : "scc", "memory", "s92", "v226", "s100", "s101");
  STAMP(5);

  {
    const u32 g = l >> 4, n = l & 15, q = (l >> 2) & 3, p = l & 3;
    u32 gd[5], gx[5];
#pragma unroll
    for (int i = 0; i < 5; i++) {
      const u32 e = 64 * i + l, ro = (e * 3641) >> 16;
      gd[i] = ro * (1152 - 288) + e * 16;
      gx[i] = gd[i] + 18432 + (ro >> 1) * 18432;
    }
    const u32 hA = l >> 5, d = l & 31;
    const u32 v0 = LKV + (d < 16 ? 1024 + hA * 512 + 16 * d : d < 18 ? 2176 - 256 + hA * 64 + 16 * d : 2304);
    const u32 one = (n & 1) ? 0x3f800000u : 0x3f80u, sel = g == (n >> 2) ? one : 0u;
    lds_u32x4* st = (lds_u32x4*)(LT + l * 16);
    st[256] = u32x4{gd[0], gd[1], gd[2], gd[3]};
    st[320] = u32x4{gd[4], (4 * g + q) * 288 + 8 * p, LKV + 2304 + 2320 * (l & 7), l < 41 ? l * 128 : OOB};
    st[384] = u32x4{LKV + l * 16, v0, v0 + (d < 16 ? 256 : d < 18 ? 32 : 0), LKV + hA * 2320 + 2048 + 4 * d};
    st[448] = u32x4{gx[0], gx[1], gx[2], gx[3]};
    st[512] = u32x4{gx[4], (n & 2) ? 0u : sel, (n & 2) ? sel : 0u, 1152 * n + 8 * g};
    st[576] = u32x4{(g >> 1) * 1152 + (g & 1) * 512 + 16 * n, 2304 * g + 1024 + 4 * n, (l < 32 && p == 0) ? 1024 + 4 * l : OOB, (g >> 1) * 4608 + (4 * (g & 1) + q) * 288 + 8 * p};
  }
  sD[14] = __builtin_amdgcn_readfirstlane(ev[0]); sD[15] = __builtin_amdgcn_readfirstlane(ev[1]);
  STAMP(6);
  asm volatile(
"s_nop 1\n"
"s_waitcnt vmcnt(6)\n"
"buffer_store_dwordx2 v[230:231], v252, s[20:23], s74 offen sc1\n"
"s_add_u32 m0, s91, 16384\n"
"s_add_u32 s92, s11, 1179648\n"
"buffer_load_dwordx4 v239, s[24:27], s92 offen lds\n"
"s_add_u32 m0, s91, 20480\n"
"s_add_u32 s92, s11, 1474560\n"
"buffer_load_dwordx4 v239, s[24:27], s92 offen lds\n"
"s_add_u32 m0, s91, 81920\n"
"s_add_u32 s92, s11, 589824\n"
"buffer_load_dwordx4 v239, s[28:31], s92 offen lds\n"
"s_add_u32 m0, s91, 86016\n"
"s_add_u32 s92, s11, 884736\n"
"buffer_load_dwordx4 v239, s[28:31], s92 offen lds\n"
"s_add_u32 m0, s91, 24576\n"
"s_add_u32 s92, s12, 0\n"
"buffer_load_dwordx4 v239, s[24:27], s92 offen lds\n"
"s_add_u32 m0, s91, 28672\n"
"s_add_u32 s92, s12, 294912\n"
"buffer_load_dwordx4 v239, s[24:27], s92 offen lds\n"
"s_add_u32 m0, s91, 32768\n"
"s_add_u32 s92, s12, 589824\n"
"buffer_load_dwordx4 v239, s[24:27], s92 offen lds\n"
"s_add_u32 m0, s91, 36864\n"
"s_add_u32 s92, s12, 884736\n"
"buffer_load_dwordx4 v239, s[24:27], s92 offen lds\n"
"s_add_u32 m0, s91, 90112\n"
"s_add_u32 s92, s11, 1179648\n"
"buffer_load_dwordx4 v239, s[28:31], s92 offen lds\n"
"s_add_u32 m0, s91, 94208\n"
"s_add_u32 s92, s11, 1474560\n"
"buffer_load_dwordx4 v239, s[28:31], s92 offen lds\n"
"s_add_u32 m0, s91, 40960\n"
"s_add_u32 s92, s12, 1179648\n"
"buffer_load_dwordx4 v239, s[24:27], s92 offen lds\n"
"s_add_u32 m0, s91, 45056\n"
"s_add_u32 s92, s12, 1474560\n"
"buffer_load_dwordx4 v239, s[24:27], s92 offen lds\n"
      : "+v"(l), "+{v[232:235]}"(vl), "+{v236}"(tru), "+{v[240:243]}"(pf), "+{v[244:245]}"(kw), "+{v[248:251]}"(sg), "+{v252}"(vfl)
      : "{s[52:67]}"(sC), "{s[68:83]}"(sD), "{s[84:87]}"(sF), "{s[88:89]}"(sG), "{v[230:231]}"(ev), "{v239}"(gu), "{s[8:15]}"(sX), "{s91}"(mu), "{s[16:31]}"(dA), "{s[4:5]}"(sP)
      : "scc", "memory", "s92", "v226");
  STAMP(7);
  asm volatile(
".macro RC d,s\n"
"v_rcp_f32 v\\d, v\\s\n"
".endm\n"
".macro MU d,a,b\n"
"v_mul_f32 v\\d, v\\a, v\\b\n"
".endm\n"
".macro MS d,a,b\n"
"v_mul_f32 v\\d, s\\a, v\\b\n"
".endm\n"
".macro FS d,a,s\n"
"v_fma_f32 v\\d, v\\a, v\\a, s\\s\n"
".endm\n"
".macro A1 d,a\n"
"v_add_f32 v\\d, 1.0, v\\a\n"
".endm\n"
".macro CV d,a,b\n"
"v_cvt_pk_bf16_f32 v\\d, v\\a, v\\b\n"
".endm\n"
".macro WL n\n"
"s_waitcnt lgkmcnt(\\n)\n"
".endm\n"
".macro WV n\n"
"s_waitcnt vmcnt(\\n)\n"
".endm\n"
".macro L v,r,s\n"
"buffer_load_dwordx4 v\\v, s[\\r:\\r+3], s\\s offen lds\n"
".endm\n"
".macro Gzvva d,a,b\n"
"v_mfma_f32_16x16x32_bf16 v[\\d:\\d+3], v[\\a:\\a+3], a[\\b:\\b+3], 0\n"
".endm\n"
".macro Gvva d,a,b\n"
"v_mfma_f32_16x16x32_bf16 v[\\d:\\d+3], v[\\a:\\a+3], a[\\b:\\b+3], v[\\d:\\d+3]\n"
".endm\n"
".macro Gvvv d,a,b\n"
"v_mfma_f32_16x16x32_bf16 v[\\d:\\d+3], v[\\a:\\a+3], v[\\b:\\b+3], v[\\d:\\d+3]\n"
".endm\n"
".macro Gzvav d,a,b\n"
"v_mfma_f32_16x16x32_bf16 v[\\d:\\d+3], a[\\a:\\a+3], v[\\b:\\b+3], 0\n"
".endm\n"
".macro Gvav d,a,b\n"
"v_mfma_f32_16x16x32_bf16 v[\\d:\\d+3], a[\\a:\\a+3], v[\\b:\\b+3], v[\\d:\\d+3]\n"
".endm\n"
".macro Gzvvv d,a,b\n"
"v_mfma_f32_16x16x32_bf16 v[\\d:\\d+3], v[\\a:\\a+3], v[\\b:\\b+3], 0\n"
".endm\n"
".macro Nzvaa d,a,b\n"
"v_mfma_f32_32x32x8_bf16 v[\\d:\\d+15], a[\\a:\\a+1], a[\\b:\\b+1], 0\n"
".endm\n"
".macro Mvaa d,a,b\n"
"v_mfma_f32_32x32x16_bf16 v[\\d:\\d+15], a[\\a:\\a+3], a[\\b:\\b+3], v[\\d:\\d+15]\n"
".endm\n"
".macro Gzvaa d,a,b\n"
"v_mfma_f32_16x16x32_bf16 v[\\d:\\d+3], a[\\a:\\a+3], a[\\b:\\b+3], 0\n"
".endm\n"
".macro Gvaa d,a,b\n"
"v_mfma_f32_16x16x32_bf16 v[\\d:\\d+3], a[\\a:\\a+3], a[\\b:\\b+3], v[\\d:\\d+3]\n"
".endm\n"
".macro E d,s\n"
"v_exp_f32 v\\d, v\\s\n"
".endm\n"
".macro Svav d,a,b,i\n"
"v_smfmac_f32_32x32x32_bf16 v[\\d:\\d+15], a[\\a:\\a+3], v[\\b:\\b+7], v\\i\n"
".endm\n"
".macro Tv d,a,o\n"
"ds_read_b64_tr_b16 v[\\d:\\d+1], v\\a offset:\\o\n"
".endm\n"
".macro Ta d,a,o\n"
"ds_read_b64_tr_b16 a[\\d:\\d+1], v\\a offset:\\o\n"
".endm\n"
".macro EN d,s\n"
"v_exp_f32 v\\d, -v\\s\n"
".endm\n"
".macro Gzaaa d,a,b\n"
"v_mfma_f32_16x16x32_bf16 a[\\d:\\d+3], a[\\a:\\a+3], a[\\b:\\b+3], 0\n"
".endm\n"
".macro Gava d,a,b\n"
"v_mfma_f32_16x16x32_bf16 a[\\d:\\d+3], v[\\a:\\a+3], a[\\b:\\b+3], a[\\d:\\d+3]\n"
".endm\n"
".macro Kaaa d,a,b\n"
"v_mfma_f32_16x16x16_bf16 a[\\d:\\d+3], a[\\a:\\a+1], a[\\b:\\b+1], a[\\d:\\d+3]\n"
".endm\n"
"s_nop 4\n"
"s_add_u32 m0, s61, 49152\n"
"s_add_u32 s53, s13, 0\n"
"L 239,24,53\n"
"s_add_u32 m0, s61, 53248\n"
"s_add_u32 s53, s13, 294912\n"
"L 239,24,53\n"
"s_add_u32 m0, s61, 98304\n"
"s_add_u32 s53, s12, 0\n"
"L 239,28,53\n"
"s_add_u32 m0, s61, 102400\n"
"s_add_u32 s53, s12, 294912\n"
"L 239,28,53\n"
"s_add_u32 m0, s61, 57344\n"
"s_add_u32 s53, s13, 589824\n"
"L 239,24,53\n"
"s_add_u32 m0, s61, 61440\n"
"s_add_u32 s53, s13, 884736\n"
"L 239,24,53\n"
"s_add_u32 m0, s61, 65536\n"
"s_add_u32 s53, s13, 1179648\n"
"L 239,24,53\n"
"s_add_u32 m0, s61, 69632\n"
"s_add_u32 s53, s13, 1474560\n"
"L 239,24,53\n"
"s_add_u32 m0, s61, 106496\n"
"s_add_u32 s53, s12, 589824\n"
"L 239,28,53\n"
"s_add_u32 m0, s61, 110592\n"
"s_add_u32 s53, s12, 884736\n"
"L 239,28,53\n"
"s_add_u32 m0, s61, 114688\n"
"s_add_u32 s53, s12, 1179648\n"
"L 239,28,53\n"
"s_add_u32 m0, s61, 118784\n"
"s_add_u32 s53, s12, 1474560\n"
"L 239,28,53\n"
"s_mov_b32 s92, 0x409d30fc\n"
"s_mov_b32 s90, 0x3ffff\n"
"s_mov_b32 s91, 0x3ffff\n"
"s_mov_b32 s98, 0x3fc62901\n"
"s_mov_b32 s52, s14\n"
"s_mov_b32 s99, 0x3f803f80\n"
"v_add_u32 v246, s61, v232\n"
"v_add_u32 v247, 65536, v246\n"
"v_lshlrev_b32 v240, 4, v233\n"
"v_and_b32 v241, 0xfff, v240\n"
"s_and_b32 s93, s79, 0xfffff000\n"
"v_and_b32 v253, 0x1ff, v235\n"
"s_lshl3_add_u32 s56, s14, s15\n"
"s_lshl2_add_u32 s56, s56, s55\n"
"s_mul_i32 s57, s56, 5248\n"
"s_lshl_b32 s56, s56, 10\n"
"s_lshl_b32 s100, s14, 8\n"
"s_sub_u32 s100, s4, s100\n"
"s_lshr_b32 s101, s100, 3\n"
"s_mul_i32 s58, s101, 3072\n"
"s_add_u32 s58, s58, 2490368\n"
"buffer_load_dwordx2 v[228:229], v235, s[20:23], s77 offen sc1\n"
"WV 0\n"
"v_cmp_ne_u64 vcc, s[82:83], v[228:229]\n"
"s_cbranch_vccnz SLX%=\n"
"BKX%=:\n"
"buffer_load_dwordx4 a[0:3], v232, s[20:23], s8 offen sc1\n"
"buffer_load_dwordx4 a[4:7], v232, s[20:23], s8 offen offset:1024 sc1\n"
"buffer_load_dwordx4 a[8:11], v232, s[20:23], s8 offen offset:2048 sc1\n"
"buffer_load_dwordx4 a[12:15], v232, s[20:23], s8 offen offset:3072 sc1\n"
"s_add_u32 s8, s8, 4096\n"
"buffer_load_dwordx4 a[16:19], v232, s[20:23], s8 offen sc1\n"
"buffer_load_dwordx4 a[20:23], v232, s[20:23], s8 offen offset:1024 sc1\n"
"buffer_load_dwordx4 a[24:27], v232, s[20:23], s8 offen offset:2048 sc1\n"
"buffer_load_dwordx4 a[28:31], v232, s[20:23], s8 offen offset:3072 sc1\n"
"s_add_u32 s8, s8, 4096\n"
"buffer_load_dwordx4 a[32:35], v232, s[20:23], s8 offen sc1\n"
"buffer_load_dwordx4 a[36:39], v232, s[20:23], s8 offen offset:1024 sc1\n"
"buffer_load_dwordx4 a[40:43], v232, s[20:23], s8 offen offset:2048 sc1\n"
"buffer_load_dwordx4 a[44:47], v232, s[20:23], s8 offen offset:3072 sc1\n"
"s_barrier\n"
"v_xor_b32 v237, 0, v236\n"
"Tv 176,237,0\n"
"Tv 178,237,512\n"
"Tv 180,237,4096\n"
"Tv 182,237,4608\n"
"v_xor_b32 v238, 32, v236\n"
"Tv 184,238,0\n"
"Tv 186,238,512\n"
"Tv 188,238,4096\n"
"Tv 190,238,4608\n"
"s_waitcnt vmcnt(8) lgkmcnt(6)\n"
"Gzvva 44,176,12\n"
"v_xor_b32 v237, 64, v236\n"
"s_waitcnt vmcnt(4) lgkmcnt(4)\n"
"Gvva 44,180,28\n"
"Tv 192,237,0\n"
"v_xor_b32 v238, 96, v236\n"
"Gzvva 40,180,24\n"
"s_add_u32 s8, s8, 4096\n"
"buffer_load_dwordx4 a[48:51], v232, s[20:23], s8 offen sc1\n"
"Gvva 40,176,8\n"
"Tv 194,237,512\n"
"Gzvva 36,176,4\n"
"Tv 196,237,4096\n"
"Gvva 36,180,20\n"
"Tv 198,237,4608\n"
"v_xor_b32 v237, 73728, v236\n"
"Gzvva 32,180,16\n"
"buffer_load_dwordx4 a[52:55], v232, s[20:23], s8 offen offset:1024 sc1\n"
"Gvva 32,176,0\n"
"WL 4\n"
"Tv 200,238,0\n"
"Gzvva 48,184,0\n"
"Tv 202,238,512\n"
"Gvva 48,188,16\n"
"Tv 204,238,4096\n"
"Gzvva 52,188,20\n"
"Tv 206,238,4608\n"
"v_xor_b32 v238, 73760, v236\n"
"Gvva 52,184,4\n"
"buffer_load_dwordx4 a[56:59], v232, s[20:23], s8 offen offset:2048 sc1\n"
"Gzvva 56,184,8\n"
"Tv 208,237,0\n"
"Gvva 56,188,24\n"
"Tv 210,237,512\n"
"Gzvva 60,188,28\n"
"Tv 212,237,4096\n"
"Gvva 60,184,12\n"
"WL 7\n"
"Tv 214,237,4608\n"
"v_xor_b32 v237, 73792, v236\n"
"Gzvva 76,192,12\n"
"buffer_load_dwordx4 a[60:63], v232, s[20:23], s8 offen offset:3072 sc1\n"
"Gvva 76,196,28\n"
"Tv 216,238,0\n"
"Gzvva 72,196,24\n"
"Tv 218,238,512\n"
"Gvva 72,192,8\n"
"Tv 220,238,4096\n"
"Gzvva 68,192,4\n"
"Tv 222,238,4608\n"
"v_xor_b32 v238, 73824, v236\n"
"Gvva 68,196,20\n"
"s_add_u32 s8, s8, 4096\n"
"buffer_load_dwordx4 a[64:67], v232, s[20:23], s8 offen sc1\n"
"Gzvva 64,196,16\n"
"Tv 180,237,4096\n"
"Gvva 64,192,0\n"
"WL 9\n"
"Tv 182,237,4608\n"
"Gzvva 80,200,0\n"
"Tv 176,237,0\n"
"Gvva 80,204,16\n"
"Tv 178,237,512\n"
"v_xor_b32 v237, 8192, v236\n"
"Gzvva 84,204,20\n"
"buffer_load_dwordx4 a[68:71], v232, s[20:23], s8 offen offset:1024 sc1\n"
"Gvva 84,200,4\n"
"Gzvva 88,200,8\n"
"Tv 188,238,4096\n"
"Gvva 88,204,24\n"
"Tv 190,238,4608\n"
"Gzvva 92,204,28\n"
"Tv 184,238,0\n"
"Gvva 92,200,12\n"
"WL 11\n"
"buffer_load_dwordx4 a[72:75], v232, s[20:23], s8 offen offset:2048 sc1\n"
"Gzvva 108,208,12\n"
"Tv 186,238,512\n"
"v_xor_b32 v238, 8224, v236\n"
"Gvva 108,212,28\n"
"Gzvva 104,212,24\n"
"Gvva 104,208,8\n"
"Gzvva 100,208,4\n"
"buffer_load_dwordx4 a[76:79], v232, s[20:23], s8 offen offset:3072 sc1\n"
"Gvva 100,212,20\n"
"Tv 196,237,4096\n"
"Gzvva 96,212,16\n"
"Tv 198,237,4608\n"
"Gvva 96,208,0\n"
"WL 10\n"
"Tv 192,237,0\n"
"Gzvva 112,216,0\n"
"Tv 194,237,512\n"
"v_xor_b32 v237, 8256, v236\n"
"Gvva 112,220,16\n"
"s_add_u32 s8, s8, 4096\n"
"buffer_load_dwordx4 a[80:83], v232, s[20:23], s8 offen sc1\n"
"Gzvva 116,220,20\n"
"Gvva 116,216,4\n"
"Gzvva 120,216,8\n"
"Tv 204,238,4096\n"
"Gvva 120,220,24\n"
"Tv 206,238,4608\n"
"Gzvva 124,220,28\n"
"buffer_load_dwordx4 a[84:87], v232, s[20:23], s8 offen offset:1024 sc1\n"
"Gvva 124,216,12\n"
"WL 10\n"
"Tv 200,238,0\n"
"Gzvva 140,176,12\n"
"Tv 202,238,512\n"
"v_xor_b32 v238, 8288, v236\n"
"Gvva 140,180,28\n"
"Gzvva 136,180,24\n"
"Gvva 136,176,8\n"
"buffer_load_dwordx4 a[88:91], v232, s[20:23], s8 offen offset:2048 sc1\n"
"Gzvva 132,176,4\n"
"Tv 212,237,4096\n"
"Gvva 132,180,20\n"
"Tv 214,237,4608\n"
"Gzvva 128,180,16\n"
"Tv 208,237,0\n"
"Gvva 128,176,0\n"
"WL 11\n"
"Tv 210,237,512\n"
"v_xor_b32 v237, 16384, v236\n"
"Gzvva 144,184,0\n"
"buffer_load_dwordx4 a[92:95], v232, s[20:23], s8 offen offset:3072 sc1\n"
"Gvva 144,188,16\n"
"Gzvva 148,188,20\n"
"Gvva 148,184,4\n"
"Gzvva 152,184,8\n"
"Tv 220,238,4096\n"
"Gvva 152,188,24\n"
"buffer_load_dwordx4 a[96:99], v232, s[20:23], s9 offen sc1\n"
"Gzvva 156,188,28\n"
"Tv 222,238,4608\n"
"Gvva 156,184,12\n"
"s_waitcnt vmcnt(9) lgkmcnt(10)\n"
"Tv 216,238,0\n"
"Gvva 44,192,44\n"
"Tv 218,238,512\n"
"v_xor_b32 v238, 16416, v236\n"
"Gvva 44,196,60\n"
"Gvva 40,196,56\n"
"buffer_load_dwordx4 a[100:103], v232, s[20:23], s9 offen offset:1024 sc1\n"
"Gvva 40,192,40\n"
"Gvva 36,192,36\n"
"Tv 180,237,4096\n"
"Gvva 36,196,52\n"
"Tv 182,237,4608\n"
"Gvva 32,196,48\n"
"Tv 176,237,0\n"
"Gvva 32,192,32\n"
"WL 11\n"
"buffer_load_dwordx4 a[104:107], v232, s[20:23], s9 offen offset:2048 sc1\n"
"Gvva 48,200,32\n"
"Tv 178,237,512\n"
"v_xor_b32 v237, 16448, v236\n"
"Gvva 48,204,48\n"
"Gvva 52,204,52\n"
"Gvva 52,200,36\n"
"Gvva 56,200,40\n"
"buffer_load_dwordx4 a[108:111], v232, s[20:23], s9 offen offset:3072 sc1\n"
"Gvva 56,204,56\n"
"Tv 188,238,4096\n"
"Gvva 60,204,60\n"
"Tv 190,238,4608\n"
"Gvva 60,200,44\n"
"WL 10\n"
"Tv 184,238,0\n"
"Gvva 76,208,44\n"
"Tv 186,238,512\n"
"v_xor_b32 v238, 16480, v236\n"
"Gvva 76,212,60\n"
"s_add_u32 s9, s9, 4096\n"
"buffer_load_dwordx4 a[112:115], v232, s[20:23], s9 offen sc1\n"
"Gvva 72,212,56\n"
"Gvva 72,208,40\n"
"Gvva 68,208,36\n"
"Tv 196,237,4096\n"
"Gvva 68,212,52\n"
"Tv 198,237,4608\n"
"Gvva 64,212,48\n"
"buffer_load_dwordx4 a[116:119], v232, s[20:23], s9 offen offset:1024 sc1\n"
"Gvva 64,208,32\n"
"WL 10\n"
"Tv 192,237,0\n"
"Gvva 80,216,32\n"
"Tv 194,237,512\n"
"v_xor_b32 v237, 81920, v236\n"
"Gvva 80,220,48\n"
"Gvva 84,220,52\n"
"Gvva 84,216,36\n"
"buffer_load_dwordx4 a[120:123], v232, s[20:23], s9 offen offset:2048 sc1\n"
"Gvva 88,216,40\n"
"Tv 204,238,4096\n"
"Gvva 88,220,56\n"
"Tv 206,238,4608\n"
"Gvva 92,220,60\n"
"Tv 200,238,0\n"
"Gvva 92,216,44\n"
"s_waitcnt vmcnt(7) lgkmcnt(11)\n"
"Tv 202,238,512\n"
"v_xor_b32 v238, 81952, v236\n"
"Gvva 44,176,76\n"
"buffer_load_dwordx4 a[124:127], v232, s[20:23], s9 offen offset:3072 sc1\n"
"Gvva 44,180,92\n"
"Gvva 40,180,88\n"
"Gvva 40,176,72\n"
"Gvva 36,176,68\n"
"Tv 212,237,4096\n"
"Gvva 36,180,84\n"
"Tv 214,237,4608\n"
"Gvva 32,180,80\n"
"s_add_u32 s9, s9, 4096\n"
"buffer_load_dwordx4 a[128:131], v232, s[20:23], s9 offen sc1\n"
"Gvva 32,176,64\n"
"WL 10\n"
"Tv 208,237,0\n"
"Gvva 48,184,64\n"
"Tv 210,237,512\n"
"v_xor_b32 v237, 81984, v236\n"
"Gvva 48,188,80\n"
"Gvva 52,188,84\n"
"buffer_load_dwordx4 a[132:135], v232, s[20:23], s9 offen offset:1024 sc1\n"
"Gvva 52,184,68\n"
"Gvva 56,184,72\n"
"Tv 220,238,4096\n"
"Gvva 56,188,88\n"
"Tv 222,238,4608\n"
"Gvva 60,188,92\n"
"Tv 216,238,0\n"
"Gvva 60,184,76\n"
"WL 11\n"
"buffer_load_dwordx4 a[136:139], v232, s[20:23], s9 offen offset:2048 sc1\n"
"Gvva 76,192,76\n"
"Tv 218,238,512\n"
"v_xor_b32 v238, 82016, v236\n"
"Gvva 76,196,92\n"
"Gvva 72,196,88\n"
"Gvva 72,192,72\n"
"Gvva 68,192,68\n"
"buffer_load_dwordx4 a[140:143], v232, s[20:23], s9 offen offset:3072 sc1\n"
"Gvva 68,196,84\n"
"Tv 180,237,4096\n"
"Gvva 64,196,80\n"
"Tv 182,237,4608\n"
"Gvva 64,192,64\n"
"WL 10\n"
"Tv 176,237,0\n"
"Gvva 80,200,64\n"
"Tv 178,237,512\n"
"v_xor_b32 v237, 24576, v236\n"
"Gvva 80,204,80\n"
"s_add_u32 s9, s9, 4096\n"
"buffer_load_dwordx4 a[144:147], v232, s[20:23], s9 offen sc1\n"
"Gvva 84,204,84\n"
"Gvva 84,200,68\n"
"Gvva 88,200,72\n"
"Tv 188,238,4096\n"
"Gvva 88,204,88\n"
"Tv 190,238,4608\n"
"Gvva 92,204,92\n"
"buffer_load_dwordx4 a[148:151], v232, s[20:23], s9 offen offset:1024 sc1\n"
"Gvva 92,200,76\n"
"WL 10\n"
"Tv 184,238,0\n"
"Gvva 108,208,44\n"
"Tv 186,238,512\n"
"v_xor_b32 v238, 24608, v236\n"
"Gvva 108,212,60\n"
"Gvva 104,212,56\n"
"Gvva 104,208,40\n"
"buffer_load_dwordx4 a[152:155], v232, s[20:23], s9 offen offset:2048 sc1\n"
"Gvva 100,208,36\n"
"Tv 196,237,4096\n"
"Gvva 100,212,52\n"
"Tv 198,237,4608\n"
"Gvva 96,212,48\n"
"Tv 192,237,0\n"
"Gvva 96,208,32\n"
"WL 11\n"
"Tv 194,237,512\n"
"v_xor_b32 v237, 24640, v236\n"
"Gvva 112,216,32\n"
"buffer_load_dwordx4 a[156:159], v232, s[20:23], s9 offen offset:3072 sc1\n"
"Gvva 112,220,48\n"
"Gvva 116,220,52\n"
"Gvva 116,216,36\n"
"Gvva 120,216,40\n"
"Tv 204,238,4096\n"
"Gvva 120,220,56\n"
"s_add_u32 s9, s9, 4096\n"
"buffer_load_dwordx4 a[160:163], v232, s[20:23], s9 offen sc1\n"
"Gvva 124,220,60\n"
"Tv 206,238,4608\n"
"Gvva 124,216,44\n"
"WL 10\n"
"Tv 200,238,0\n"
"Gvva 140,176,44\n"
"Tv 202,238,512\n"
"v_xor_b32 v238, 24672, v236\n"
"Gvva 140,180,60\n"
"Gvva 136,180,56\n"
"buffer_load_dwordx4 a[164:167], v232, s[20:23], s9 offen offset:1024 sc1\n"
"Gvva 136,176,40\n"
"Gvva 132,176,36\n"
"Tv 212,237,4096\n"
"Gvva 132,180,52\n"
"Tv 214,237,4608\n"
"Gvva 128,180,48\n"
"Tv 208,237,0\n"
"Gvva 128,176,32\n"
"WL 11\n"
"buffer_load_dwordx4 a[168:171], v232, s[20:23], s9 offen offset:2048 sc1\n"
"Gvva 144,184,32\n"
"Tv 210,237,512\n"
"v_xor_b32 v237, 32768, v236\n"
"Gvva 144,188,48\n"
"Gvva 148,188,52\n"
"Gvva 148,184,36\n"
"Gvva 152,184,40\n"
"buffer_load_dwordx4 a[172:175], v232, s[20:23], s9 offen offset:3072 sc1\n"
"Gvva 152,188,56\n"
"Tv 220,238,4096\n"
"Gvva 156,188,60\n"
"Tv 222,238,4608\n"
"Gvva 156,184,44\n"
"s_waitcnt vmcnt(12) lgkmcnt(10)\n"
"Tv 216,238,0\n"
"Gvva 44,192,108\n"
"Tv 218,238,512\n"
"v_xor_b32 v238, 32800, v236\n"
"Gvva 44,196,124\n"
"Gvva 40,196,120\n"
"s_add_u32 s9, s9, 4096\n"
"buffer_load_dwordx4 a[176:179], v232, s[20:23], s9 offen sc1\n"
"Gvva 40,192,104\n"
"Gvva 36,192,100\n"
"Tv 180,237,4096\n"
"Gvva 36,196,116\n"
"Tv 182,237,4608\n"
"Gvva 32,196,112\n"
"buffer_load_dwordx4 a[180:183], v232, s[20:23], s9 offen offset:1024 sc1\n"
"Gvva 32,192,96\n"
"WL 10\n"
"Tv 176,237,0\n"
"Gvva 48,200,96\n"
"Tv 178,237,512\n"
"v_xor_b32 v237, 32832, v236\n"
"Gvva 48,204,112\n"
"Gvva 52,204,116\n"
"Gvva 52,200,100\n"
"buffer_load_dwordx4 a[184:187], v232, s[20:23], s9 offen offset:2048 sc1\n"
"Gvva 56,200,104\n"
"Tv 188,238,4096\n"
"Gvva 56,204,120\n"
"Tv 190,238,4608\n"
"Gvva 60,204,124\n"
"Tv 184,238,0\n"
"Gvva 60,200,108\n"
"WL 11\n"
"Tv 186,238,512\n"
"v_xor_b32 v238, 32864, v236\n"
"Gvva 76,208,108\n"
"buffer_load_dwordx4 a[188:191], v232, s[20:23], s9 offen offset:3072 sc1\n"
"Gvva 76,212,124\n"
"Gvva 72,212,120\n"
"Gvva 72,208,104\n"
"Gvva 68,208,100\n"
"Tv 196,237,4096\n"
"Gvva 68,212,116\n"
"buffer_load_dwordx4 a[192:195], v232, s[20:23], s10 offen sc1\n"
"Gvva 64,212,112\n"
"Tv 198,237,4608\n"
"Gvva 64,208,96\n"
"WL 10\n"
"Tv 192,237,0\n"
"Gvva 80,216,96\n"
"Tv 194,237,512\n"
"v_xor_b32 v237, 90112, v236\n"
"Gvva 80,220,112\n"
"Gvva 84,220,116\n"
"buffer_load_dwordx4 a[196:199], v232, s[20:23], s10 offen offset:1024 sc1\n"
"Gvva 84,216,100\n"
"Gvva 88,216,104\n"
"Tv 204,238,4096\n"
"Gvva 88,220,120\n"
"Tv 206,238,4608\n"
"Gvva 92,220,124\n"
"Tv 200,238,0\n"
"Gvva 92,216,108\n"
"s_waitcnt vmcnt(10) lgkmcnt(11)\n"
"buffer_load_dwordx4 a[200:203], v232, s[20:23], s10 offen offset:2048 sc1\n"
"Gvva 44,176,140\n"
"Tv 202,238,512\n"
"v_xor_b32 v238, 90144, v236\n"
"Gvva 44,180,156\n"
"Gvva 40,180,152\n"
"Gvva 40,176,136\n"
"Gvva 36,176,132\n"
"buffer_load_dwordx4 a[204:207], v232, s[20:23], s10 offen offset:3072 sc1\n"
"Gvva 36,180,148\n"
"Tv 212,237,4096\n"
"Gvva 32,180,144\n"
"Tv 214,237,4608\n"
"Gvva 32,176,128\n"
"WL 10\n"
"Tv 208,237,0\n"
"Gvva 48,184,128\n"
"Tv 210,237,512\n"
"v_xor_b32 v237, 90176, v236\n"
"Gvva 48,188,144\n"
"s_add_u32 s10, s10, 4096\n"
"buffer_load_dwordx4 a[208:211], v232, s[20:23], s10 offen sc1\n"
"Gvva 52,188,148\n"
"Gvva 52,184,132\n"
"Gvva 56,184,136\n"
"Tv 220,238,4096\n"
"Gvva 56,188,152\n"
"Tv 222,238,4608\n"
"Gvva 60,188,156\n"
"buffer_load_dwordx4 a[212:215], v232, s[20:23], s10 offen offset:1024 sc1\n"
"Gvva 60,184,140\n"
"WL 10\n"
"Tv 216,238,0\n"
"Gvva 76,192,140\n"
"Tv 218,238,512\n"
"v_xor_b32 v238, 90208, v236\n"
"Gvva 76,196,156\n"
"Gvva 72,196,152\n"
"Gvva 72,192,136\n"
"buffer_load_dwordx4 a[216:219], v232, s[20:23], s10 offen offset:2048 sc1\n"
"Gvva 68,192,132\n"
"Tv 180,237,4096\n"
"Gvva 68,196,148\n"
"Tv 182,237,4608\n"
"Gvva 64,196,144\n"
"Tv 176,237,0\n"
"Gvva 64,192,128\n"
"WL 11\n"
"Tv 178,237,512\n"
"v_xor_b32 v237, 40960, v236\n"
"Gvva 80,200,128\n"
"buffer_load_dwordx4 a[220:223], v232, s[20:23], s10 offen offset:3072 sc1\n"
"Gvva 80,204,144\n"
"Gvva 84,204,148\n"
"Gvva 84,200,132\n"
"Gvva 88,200,136\n"
"Tv 188,238,4096\n"
"Gvva 88,204,152\n"
"s_add_u32 s10, s10, 4096\n"
"buffer_load_dwordx4 a[224:227], v232, s[20:23], s10 offen sc1\n"
"Gvva 92,204,156\n"
"Tv 190,238,4608\n"
"Gvva 92,200,140\n"
"WL 10\n"
"Tv 184,238,0\n"
"Gvva 108,208,76\n"
"Tv 186,238,512\n"
"v_xor_b32 v238, 40992, v236\n"
"Gvva 108,212,92\n"
"Gvva 104,212,88\n"
"buffer_load_dwordx4 a[228:231], v232, s[20:23], s10 offen offset:1024 sc1\n"
"Gvva 104,208,72\n"
"Gvva 100,208,68\n"
"Tv 196,237,4096\n"
"Gvva 100,212,84\n"
"Tv 198,237,4608\n"
"Gvva 96,212,80\n"
"Tv 192,237,0\n"
"Gvva 96,208,64\n"
"WL 11\n"
"buffer_load_dwordx4 a[232:235], v232, s[20:23], s10 offen offset:2048 sc1\n"
"Gvva 112,216,64\n"
"Tv 194,237,512\n"
"v_xor_b32 v237, 41024, v236\n"
"Gvva 112,220,80\n"
"Gvva 116,220,84\n"
"Gvva 116,216,68\n"
"Gvva 120,216,72\n"
"buffer_load_dwordx4 a[236:239], v232, s[20:23], s10 offen offset:3072 sc1\n"
"Gvva 120,220,88\n"
"Tv 204,238,4096\n"
"Gvva 124,220,92\n"
"Tv 206,238,4608\n"
"Gvva 124,216,76\n"
"WL 10\n"
"Tv 200,238,0\n"
"Gvva 140,176,76\n"
"Tv 202,238,512\n"
"v_xor_b32 v238, 41056, v236\n"
"Gvva 140,180,92\n"
"s_add_u32 s10, s10, 4096\n"
"buffer_load_dwordx4 a[240:243], v232, s[20:23], s10 offen sc1\n"
"Gvva 136,180,88\n"
"Gvva 136,176,72\n"
"Gvva 132,176,68\n"
"Tv 212,237,4096\n"
"Gvva 132,180,84\n"
"Tv 214,237,4608\n"
"Gvva 128,180,80\n"
"buffer_load_dwordx4 a[244:247], v232, s[20:23], s10 offen offset:1024 sc1\n"
"Gvva 128,176,64\n"
"WL 10\n"
"Tv 208,237,0\n"
"Gvva 144,184,64\n"
"Tv 210,237,512\n"
"v_xor_b32 v237, 49152, v236\n"
"Gvva 144,188,80\n"
"Gvva 148,188,84\n"
"Gvva 148,184,68\n"
"buffer_load_dwordx4 a[248:251], v232, s[20:23], s10 offen offset:2048 sc1\n"
"Gvva 152,184,72\n"
"Tv 220,238,4096\n"
"Gvva 152,188,88\n"
"Tv 222,238,4608\n"
"Gvva 156,188,92\n"
"Tv 216,238,0\n"
"Gvva 156,184,76\n"
"s_waitcnt vmcnt(15) lgkmcnt(11)\n"
"Tv 218,238,512\n"
"v_xor_b32 v238, 49184, v236\n"
"Gvva 44,192,172\n"
"buffer_load_dwordx4 a[252:255], v232, s[20:23], s10 offen offset:3072 sc1\n"
"Gvva 44,196,188\n"
"Gvva 40,196,184\n"
"Gvva 40,192,168\n"
"Gvva 36,192,164\n"
"Tv 180,237,4096\n"
"Gvva 36,196,180\n"
"Tv 182,237,4608\n"
"Gvva 32,196,176\n"
"s_add_u32 s10, s10, 4096\n"
"buffer_load_dwordx4 v[0:3], v232, s[20:23], s10 offen sc1\n"
"Gvva 32,192,160\n"
"WL 10\n"
"Tv 176,237,0\n"
"Gvva 48,200,160\n"
"Tv 178,237,512\n"
"v_xor_b32 v237, 49216, v236\n"
"Gvva 48,204,176\n"
"Gvva 52,204,180\n"
"buffer_load_dwordx4 v[4:7], v232, s[20:23], s10 offen offset:1024 sc1\n"
"Gvva 52,200,164\n"
"Gvva 56,200,168\n"
"Tv 188,238,4096\n"
"Gvva 56,204,184\n"
"Tv 190,238,4608\n"
"Gvva 60,204,188\n"
"Tv 184,238,0\n"
"Gvva 60,200,172\n"
"WL 11\n"
"buffer_load_dwordx4 v[8:11], v232, s[20:23], s10 offen offset:2048 sc1\n"
"Gvva 76,208,172\n"
"Tv 186,238,512\n"
"v_xor_b32 v238, 49248, v236\n"
"Gvva 76,212,188\n"
"Gvva 72,212,184\n"
"Gvva 72,208,168\n"
"Gvva 68,208,164\n"
"buffer_load_dwordx4 v[12:15], v232, s[20:23], s10 offen offset:3072 sc1\n"
"Gvva 68,212,180\n"
"Tv 196,237,4096\n"
"Gvva 64,212,176\n"
"Tv 198,237,4608\n"
"Gvva 64,208,160\n"
"WL 10\n"
"Tv 192,237,0\n"
"Gvva 80,216,160\n"
"Tv 194,237,512\n"
"v_xor_b32 v237, 98304, v236\n"
"Gvva 80,220,176\n"
"s_add_u32 s10, s10, 4096\n"
"buffer_load_dwordx4 v[16:19], v232, s[20:23], s10 offen sc1\n"
"Gvva 84,220,180\n"
"Gvva 84,216,164\n"
"Gvva 88,216,168\n"
"Tv 204,238,4096\n"
"Gvva 88,220,184\n"
"Tv 206,238,4608\n"
"Gvva 92,220,188\n"
"buffer_load_dwordx4 v[20:23], v232, s[20:23], s10 offen offset:1024 sc1\n"
"Gvva 92,216,172\n"
"s_waitcnt vmcnt(14) lgkmcnt(10)\n"
"Tv 200,238,0\n"
"Gvva 44,176,204\n"
"Tv 202,238,512\n"
"v_xor_b32 v238, 98336, v236\n"
"Gvva 44,180,220\n"
"Gvva 40,180,216\n"
"Gvva 40,176,200\n"
"buffer_load_dwordx4 v[24:27], v232, s[20:23], s10 offen offset:2048 sc1\n"
"Gvva 36,176,196\n"
"Tv 212,237,4096\n"
"Gvva 36,180,212\n"
"Tv 214,237,4608\n"
"Gvva 32,180,208\n"
"Tv 208,237,0\n"
"Gvva 32,176,192\n"
"WL 11\n"
"Tv 210,237,512\n"
"v_xor_b32 v237, 98368, v236\n"
"Gvva 48,184,192\n"
"buffer_load_dwordx4 v[28:31], v232, s[20:23], s10 offen offset:3072 sc1\n"
"Gvva 48,188,208\n"
"Gvva 52,188,212\n"
"Gvva 52,184,196\n"
"Gvva 56,184,200\n"
"Tv 220,238,4096\n"
"v_add_u32 v228, s56, v232\n"
"Gvva 56,188,216\n"
"s_add_u32 m0, s61, 122880\n"
"Tv 222,238,4608\n"
"Gvva 60,188,220\n"
"s_add_u32 s53, s13, 0\n"
"L 239,28,53\n"
"Gvva 60,184,204\n"
"WL 10\n"
"Tv 216,238,0\n"
"Gvva 76,192,204\n"
"s_getpc_b64 s[6:7]\n"
".Lpc1:\n"
"Tv 218,238,512\n"
"v_xor_b32 v238, 98400, v236\n"
"Gvva 76,196,220\n"
"v_min_u32 v228, .Lend-.Lpc1-4, v228\n"
"Gvva 72,196,216\n"
"s_add_u32 m0, s61, 126976\n"
"global_load_dword v229, v228, s[6:7]\n"
"Gvva 72,192,200\n"
"s_add_u32 s53, s13, 294912\n"
"L 239,28,53\n"
"Gvva 68,192,196\n"
"Tv 180,237,4096\n"
"Gvva 68,196,212\n"
"Tv 182,237,4608\n"
"Gvva 64,196,208\n"
"Tv 176,237,0\n"
"Gvva 64,192,192\n"
"WL 11\n"
"Tv 178,237,512\n"
"v_xor_b32 v237, 57344, v236\n"
"Gvva 80,200,192\n"
"s_add_u32 m0, s61, 131072\n"
"Gvva 80,204,208\n"
"s_add_u32 s53, s13, 589824\n"
"L 239,28,53\n"
"Gvva 84,204,212\n"
"Gvva 84,200,196\n"
"Gvva 88,200,200\n"
"s_add_u32 m0, s61, 135168\n"
"Tv 188,238,4096\n"
"Gvva 88,204,216\n"
"s_add_u32 s53, s13, 884736\n"
"L 239,28,53\n"
"Gvva 92,204,220\n"
"Tv 190,238,4608\n"
"Gvva 92,200,204\n"
"WL 10\n"
"Tv 184,238,0\n"
"Gvva 108,208,108\n"
"Tv 186,238,512\n"
"v_xor_b32 v238, 57376, v236\n"
"Gvva 108,212,124\n"
"s_add_u32 m0, s61, 139264\n"
"Gvva 104,212,120\n"
"s_add_u32 s53, s13, 1179648\n"
"L 239,28,53\n"
"Gvva 104,208,104\n"
"Gvva 100,208,100\n"
"Tv 196,237,4096\n"
"Gvva 100,212,116\n"
"Tv 198,237,4608\n"
"Gvva 96,212,112\n"
"s_add_u32 m0, s61, 143360\n"
"Tv 192,237,0\n"
"Gvva 96,208,96\n"
"WL 11\n"
"Tv 194,237,512\n"
"v_xor_b32 v237, 57408, v236\n"
"Gvva 112,216,96\n"
"s_add_u32 s53, s13, 1474560\n"
"L 239,28,53\n"
"Gvva 112,220,112\n"
"Gvva 116,220,116\n"
"Gvva 116,216,100\n"
"Gvva 120,216,104\n"
"buffer_load_dwordx4 v[164:167], v161, s[16:19], s101 offen offset:1152\n"
"Gvva 120,220,120\n"
"Tv 204,238,4096\n"
"Gvva 124,220,124\n"
"Tv 206,238,4608\n"
"Gvva 124,216,108\n"
"WL 10\n"
"Tv 200,238,0\n"
"Gvva 140,176,108\n"
"Tv 202,238,512\n"
"v_xor_b32 v238, 57440, v236\n"
"Gvva 140,180,124\n"
"buffer_load_dwordx4 v[168:171], v161, s[16:19], s101 offen offset:2304\n"
"Gvva 136,180,120\n"
"Gvva 136,176,104\n"
"Gvva 132,176,100\n"
"Tv 212,237,4096\n"
"Gvva 132,180,116\n"
"Tv 214,237,4608\n"
"Gvva 128,180,112\n"
"buffer_load_dwordx4 v[172:175], v161, s[16:19], s101 offen\n"
"Gvva 128,176,96\n"
"WL 10\n"
"Tv 208,237,0\n"
"Gvva 144,184,96\n"
"Tv 210,237,512\n"
"v_xor_b32 v237, 65536, v236\n"
"Gvva 144,188,112\n"
"Gvva 148,188,116\n"
"Gvva 148,184,100\n"
"buffer_load_dword v229, v160, s[24:27], s100 offen offset:1024\n"
"Gvva 152,184,104\n"
"Tv 220,238,4096\n"
"Gvva 152,188,120\n"
"Tv 222,238,4608\n"
"Gvva 156,188,124\n"
"Tv 216,238,0\n"
"Gvva 156,184,108\n"
"s_waitcnt vmcnt(19) lgkmcnt(11)\n"
"Tv 218,238,512\n"
"v_xor_b32 v238, 65568, v236\n"
"Gvva 44,192,236\n"
"buffer_load_dword v229, v160, s[28:31], s100 offen offset:1024\n"
"Gvva 44,196,252\n"
"Gvva 40,196,248\n"
"Gvva 40,192,232\n"
"Gvva 36,192,228\n"
"Tv 180,237,4096\n"
"Gvva 36,196,244\n"
"Tv 182,237,4608\n"
"Gvva 32,196,240\n"
"Tv 176,237,0\n"
"Gvva 32,192,224\n"
"WL 11\n"
"Tv 178,237,512\n"
"v_xor_b32 v237, 65600, v236\n"
"Gvva 48,200,224\n"
"Gvva 48,204,240\n"
"Gvva 52,204,244\n"
"Gvva 52,200,228\n"
"Gvva 56,200,232\n"
"Tv 188,238,4096\n"
"Gvva 56,204,248\n"
"Tv 190,238,4608\n"
"Gvva 60,204,252\n"
"Tv 184,238,0\n"
"Gvva 60,200,236\n"
"WL 11\n"
"Tv 186,238,512\n"
"v_xor_b32 v238, 65632, v236\n"
"Gvva 76,208,236\n"
"Gvva 76,212,252\n"
"Gvva 72,212,248\n"
"Gvva 72,208,232\n"
"Gvva 68,208,228\n"
"Tv 196,237,4096\n"
"Gvva 68,212,244\n"
"Tv 198,237,4608\n"
"Gvva 64,212,240\n"
"Tv 192,237,0\n"
"Gvva 64,208,224\n"
"WL 11\n"
"Tv 194,237,512\n"
"v_xor_b32 v237, 106496, v236\n"
"Gvva 80,216,224\n"
"Gvva 80,220,240\n"
"Gvva 84,220,244\n"
"Gvva 84,216,228\n"
"Gvva 88,216,232\n"
"Tv 204,238,4096\n"
"Gvva 88,220,248\n"
"Tv 206,238,4608\n"
"Gvva 92,220,252\n"
"Tv 200,238,0\n"
"Gvva 92,216,236\n"
"s_waitcnt vmcnt(12) lgkmcnt(11)\n"
"Tv 202,238,512\n"
"v_xor_b32 v238, 106528, v236\n"
"Gvvv 44,176,12\n"
"s_barrier\n"
"Gvvv 44,180,28\n"
"Gvvv 40,180,24\n"
"s_add_u32 m0, s61, 0\n"
"Gvvv 40,176,8\n"
"Gvvv 36,176,4\n"
"s_add_u32 s53, s11, 128\n"
"L 239,24,53\n"
"s_nop 1\n"
"FS 224,44,92\n"
"Gvvv 36,180,20\n"
"Tv 212,237,4096\n"
"MU 224,224,44\n"
"Gvvv 32,180,16\n"
"s_add_u32 m0, s61, 4096\n"
"Tv 214,237,4608\n"
"FS 225,45,92\n"
"Gvvv 32,176,0\n"
"WL 10\n"
"Tv 208,237,0\n"
"MU 225,225,45\n"
"Gvvv 48,184,0\n"
"s_add_u32 s53, s11, 295040\n"
"L 239,24,53\n"
"FS 226,46,92\n"
"Gvvv 48,188,16\n"
"Tv 210,237,512\n"
"MU 226,226,46\n"
"Gvvv 52,188,20\n"
"Tv 220,237,12288\n"
"FS 227,47,92\n"
"Gvvv 52,184,4\n"
"s_add_u32 m0, s61, 8192\n"
"Tv 222,237,12800\n"
"MU 227,227,47\n"
"Gvvv 56,184,8\n"
"s_add_u32 s53, s11, 589952\n"
"L 239,24,53\n"
"FS 254,40,92\n"
"Gvvv 56,188,24\n"
"Tv 216,237,8192\n"
"MU 254,254,40\n"
"Gvvv 60,188,28\n"
"Tv 218,237,8704\n"
"v_xor_b32 v237, 106560, v236\n"
"Gvvv 60,184,12\n"
"WL 12\n"
"EN 224,224\n"
"Gvvv 76,192,12\n"
"s_add_u32 m0, s61, 12288\n"
"EN 225,225\n"
"Gvvv 76,196,28\n"
"s_add_u32 s53, s11, 884864\n"
"L 239,24,53\n"
"A1 224,224\n"
"Gvvv 72,196,24\n"
"RC 224,224\n"
"Gvvv 72,192,8\n"
"EN 226,226\n"
"Gvvv 68,192,4\n"
"Tv 180,238,4096\n"
"MU 44,224,44\n"
"Gvvv 68,196,20\n"
"s_add_u32 m0, s61, 16384\n"
"Tv 182,238,4608\n"
"A1 225,225\n"
"Gvvv 64,196,16\n"
"s_add_u32 s53, s11, 1179776\n"
"L 239,24,53\n"
"A1 226,226\n"
"Gvvv 64,192,0\n"
"WL 10\n"
"Tv 176,238,0\n"
"FS 251,41,92\n"
"Gvvv 80,200,0\n"
"Tv 178,238,512\n"
"MU 251,251,41\n"
"Gvvv 80,204,16\n"
"Tv 188,238,12288\n"
"FS 224,42,92\n"
"Gvvv 84,204,20\n"
"s_add_u32 m0, s61, 20480\n"
"Tv 190,238,12800\n"
"MU 224,224,42\n"
"Gvvv 84,200,4\n"
"s_add_u32 s53, s11, 1474688\n"
"L 239,24,53\n"
"Gvvv 88,200,8\n"
"RC 225,225\n"
"Gvvv 88,204,24\n"
"Tv 184,238,8192\n"
"MU 45,225,45\n"
"Gvvv 92,204,28\n"
"Tv 186,238,8704\n"
"v_xor_b32 v238, 106592, v236\n"
"Gvvv 92,200,12\n"
"WL 8\n"
"RC 226,226\n"
"Gvva 108,208,140\n"
"s_add_u32 m0, s61, 24576\n"
"EN 227,227\n"
"Gvva 108,212,156\n"
"s_add_u32 s53, s12, 128\n"
"L 239,24,53\n"
"MU 46,226,46\n"
"Gvva 108,216,172\n"
"EN 254,254\n"
"Gvva 108,220,188\n"
"EN 251,251\n"
"Gvva 104,220,184\n"
"s_add_u32 m0, s61, 28672\n"
"Tv 196,237,4096\n"
"A1 227,227\n"
"Gvva 104,216,168\n"
"s_add_u32 s53, s12, 295040\n"
"L 239,24,53\n"
"A1 254,254\n"
"Gvva 104,212,152\n"
"Tv 198,237,4608\n"
"A1 251,251\n"
"Gvva 104,208,136\n"
"Tv 192,237,0\n"
"FS 225,43,92\n"
"Gvva 100,208,132\n"
"Tv 194,237,512\n"
"MU 225,225,43\n"
"Gvva 100,212,148\n"
"s_add_u32 m0, s61, 32768\n"
"Tv 204,237,12288\n"
"FS 226,36,92\n"
"Gvva 100,216,164\n"
"s_add_u32 s53, s12, 589952\n"
"L 239,24,53\n"
"MU 226,226,36\n"
"Gvva 100,220,180\n"
"RC 227,227\n"
"Gvva 96,220,176\n"
"Tv 206,237,12800\n"
"MU 47,227,47\n"
"Gvva 96,216,160\n"
"Tv 200,237,8192\n"
"FS 227,37,92\n"
"Gvva 96,212,144\n"
"s_add_u32 m0, s61, 36864\n"
"Tv 202,237,8704\n"
"v_xor_b32 v237, 122880, v236\n"
"Gvva 96,208,128\n"
"WL 8\n"
"RC 254,254\n"
"Gvva 112,176,128\n"
"s_add_u32 s53, s12, 884864\n"
"L 239,24,53\n"
"RC 251,251\n"
"Gvva 112,180,144\n"
"EN 224,224\n"
"Gvva 112,184,160\n"
"EN 225,225\n"
"Gvva 112,188,176\n"
"s_add_u32 m0, s61, 40960\n"
"EN 226,226\n"
"Gvva 116,188,180\n"
"s_add_u32 s53, s12, 1179776\n"
"L 239,24,53\n"
"MU 40,254,40\n"
"Gvva 116,184,164\n"
"Tv 220,238,12288\n"
"MU 41,251,41\n"
"Gvva 116,180,148\n"
"Tv 222,238,12800\n"
"A1 224,224\n"
"Gvva 116,176,132\n"
"Tv 216,238,8192\n"
"A1 225,225\n"
"Gvva 120,176,136\n"
"s_add_u32 m0, s61, 45056\n"
"Tv 218,238,8704\n"
"A1 226,226\n"
"Gvva 120,180,152\n"
"s_add_u32 s53, s12, 1474688\n"
"L 239,24,53\n"
"MU 227,227,37\n"
"Gvva 120,184,168\n"
"Tv 212,238,4096\n"
"FS 254,38,92\n"
"Gvva 120,188,184\n"
"Tv 214,238,4608\n"
"MU 254,254,38\n"
"Gvva 124,188,188\n"
"Tv 208,238,0\n"
"FS 251,39,92\n"
"Gvva 124,184,172\n"
"s_add_u32 m0, s61, 49152\n"
"Tv 210,238,512\n"
"v_xor_b32 v238, 122912, v236\n"
"Gvva 124,180,156\n"
"s_add_u32 s53, s13, 128\n"
"L 239,24,53\n"
"MU 251,251,39\n"
"Gvva 124,176,140\n"
"s_waitcnt vmcnt(18) lgkmcnt(8)\n"
"RC 224,224\n"
"Gvva 140,192,140\n"
"s_barrier\n"
"RC 225,225\n"
"Gvva 140,196,156\n"
"RC 226,226\n"
"Gvva 140,200,172\n"
"s_add_u32 m0, s61, 53248\n"
"EN 227,227\n"
"Gvva 140,204,188\n"
"s_add_u32 s53, s13, 295040\n"
"L 239,24,53\n"
"MU 42,224,42\n"
"Gvva 136,204,184\n"
"Tv 188,237,12288\n"
"MU 43,225,43\n"
"Gvva 136,200,168\n"
"Tv 190,237,12800\n"
"MU 36,226,36\n"
"Gvva 136,196,152\n"
"Tv 184,237,8192\n"
"A1 227,227\n"
"Gvva 136,192,136\n"
"s_add_u32 m0, s61, 57344\n"
"Tv 186,237,8704\n"
"FS 224,32,92\n"
"Gvva 132,192,132\n"
"s_add_u32 s53, s13, 589952\n"
"L 239,24,53\n"
"MU 224,224,32\n"
"Gvva 132,196,148\n"
"Tv 180,237,4096\n"
"FS 225,33,92\n"
"Gvva 132,200,164\n"
"Tv 182,237,4608\n"
"MU 225,225,33\n"
"Gvva 132,204,180\n"
"Tv 176,237,0\n"
"FS 226,34,92\n"
"Gvva 128,204,176\n"
"s_add_u32 m0, s61, 61440\n"
"Tv 178,237,512\n"
"MU 226,226,34\n"
"Gvva 128,200,160\n"
"s_add_u32 s53, s13, 884864\n"
"L 239,24,53\n"
"Gvva 128,196,144\n"
"RC 227,227\n"
"Gvva 128,192,128\n"
"WL 8\n"
"Tv 196,237,20480\n"
"MU 37,227,37\n"
"Gvva 144,208,128\n"
"Tv 198,237,20992\n"
"EN 254,254\n"
"Gvva 144,212,144\n"
"s_add_u32 m0, s61, 65536\n"
"Tv 192,237,16384\n"
"A1 254,254\n"
"Gvva 144,216,160\n"
"s_add_u32 s53, s13, 1179776\n"
"L 239,24,53\n"
"FS 227,35,92\n"
"Gvva 144,220,176\n"
"WV 19\n"
"Tv 194,237,16896\n"
"v_xor_b32 v237, 122944, v236\n"
"Gvva 148,220,180\n"
"s_add_u32 s58, s58, 18432\n"
"buffer_store_dwordx4 v[164:167], v163, s[20:23], s58 offen sc1\n"
"MU 227,227,35\n"
"Gvva 148,216,164\n"
"RC 254,254\n"
"Gvva 148,212,148\n"
"s_add_u32 m0, s61, 69632\n"
"EN 251,251\n"
"Gvva 148,208,132\n"
"s_add_u32 s53, s13, 1474688\n"
"L 239,24,53\n"
"MU 38,254,38\n"
"Gvva 152,208,136\n"
"EN 224,224\n"
"Gvva 152,212,152\n"
"s_add_u32 s58, s58, 18432\n"
"buffer_store_dwordx4 v[168:171], v163, s[20:23], s58 offen sc1\n"
"A1 251,251\n"
"Gvva 152,216,168\n"
"RC 251,251\n"
"Gvva 152,220,184\n"
"EN 225,225\n"
"Gvva 156,220,188\n"
"Tv 204,238,4096\n"
"MU 39,251,39\n"
"Gvva 156,216,172\n"
"Tv 206,238,4608\n"
"A1 224,224\n"
"Gvva 156,212,156\n"
"Tv 200,238,0\n"
"A1 225,225\n"
"Gvva 156,208,140\n"
"WL 3\n"
"Tv 202,238,512\n"
"FS 254,48,92\n"
"Gvva 108,176,204\n"
"s_add_u32 s58, s58, 55296\n"
"buffer_store_dwordx4 v[172:175], v163, s[20:23], s58 offen sc1\n"
"RC 224,224\n"
"Gvva 108,180,220\n"
"Tv 220,238,20480\n"
"MU 32,224,32\n"
"Gvva 108,184,236\n"
"buffer_load_dwordx4 v[164:167], v160, s[24:27], s100 offen offset:1024\n"
"MU 254,254,48\n"
"Gvva 108,188,252\n"
"Tv 222,238,20992\n"
"FS 251,49,92\n"
"Gvvv 108,192,12\n"
"Tv 216,238,16384\n"
"MU 251,251,49\n"
"Gvvv 108,196,28\n"
"Tv 218,238,16896\n"
"FS 224,50,92\n"
"Gvvv 104,196,24\n"
"buffer_load_dwordx4 v[168:171], v160, s[28:31], s100 offen offset:1024\n"
"MU 224,224,50\n"
"Gvvv 104,192,8\n"
"RC 225,225\n"
"Gvva 104,188,248\n"
"Tv 212,238,12288\n"
"MU 33,225,33\n"
"Gvva 104,184,232\n"
"Tv 214,238,12800\n"
"MU 108,108,44\n"
"Gvva 104,180,216\n"
"buffer_load_dword v172, v162, s[16:19], s101 offen\n"
"MU 109,109,45\n"
"Gvva 104,176,200\n"
"Tv 208,238,8192\n"
"MU 110,110,46\n"
"Gvva 100,176,196\n"
"Tv 210,238,8704\n"
"v_xor_b32 v238, 122976, v236\n"
"Gvva 100,180,212\n"
"EN 226,226\n"
"Gvva 100,184,228\n"
"buffer_load_dword v173, v162, s[16:19], s101 offen offset:2304\n"
"A1 226,226\n"
"Gvva 100,188,244\n"
"RC 226,226\n"
"Gvvv 100,192,4\n"
"EN 227,227\n"
"Gvvv 100,196,20\n"
"EN 254,254\n"
"Gvvv 96,196,16\n"
"buffer_load_dword v174, v162, s[16:19], s101 offen offset:1152\n"
"MU 34,226,34\n"
"Gvvv 96,192,0\n"
"EN 251,251\n"
"Gvva 96,188,240\n"
"Tv 196,237,20480\n"
"A1 227,227\n"
"Gvva 96,184,224\n"
"Tv 198,237,20992\n"
"MU 111,111,47\n"
"Gvva 96,180,208\n"
"buffer_load_dword v175, v162, s[16:19], s101 offen offset:2304\n"
"CV 44,108,109\n"
"Gvva 96,176,192\n"
"WL 2\n"
"Tv 192,237,16384\n"
"CV 45,110,111\n"
"Gvva 112,200,192\n"
"Tv 194,237,16896\n"
"MU 104,104,40\n"
"Gvva 112,204,208\n"
"Tv 188,237,12288\n"
"MU 105,105,41\n"
"Gvva 112,208,224\n"
"Tv 190,237,12800\n"
"MU 106,106,42\n"
"Gvva 112,212,240\n"
"Tv 184,237,8192\n"
"MU 107,107,43\n"
"Gvvv 112,216,0\n"
"Tv 186,237,8704\n"
"CV 40,104,105\n"
"Gvvv 112,220,16\n"
"Tv 180,237,4096\n"
"CV 41,106,107\n"
"Gvvv 116,220,20\n"
"Tv 182,237,4608\n"
"MU 100,100,36\n"
"Gvvv 116,216,4\n"
"Tv 176,237,0\n"
"MU 101,101,37\n"
"Gvva 116,212,244\n"
"Tv 178,237,512\n"
"RC 227,227\n"
"Gvva 116,208,228\n"
"EN 224,224\n"
"Gvva 116,204,212\n"
"MU 35,227,35\n"
"MU 102,102,38\n"
"Gvva 116,200,196\n"
"MU 103,103,39\n"
"CV 36,100,101\n"
"Gvva 120,200,200\n"
"CV 37,102,103\n"
"MU 96,96,32\n"
"Gvva 120,204,216\n"
"MU 97,97,33\n"
"MU 98,98,34\n"
"Gvva 120,208,232\n"
"MU 99,99,35\n"
"CV 32,96,97\n"
"Gvva 120,212,248\n"
"CV 33,98,99\n"
"A1 254,254\n"
"Gvvv 120,216,8\n"
"RC 254,254\n"
"Gvvv 120,220,24\n"
"MU 48,254,48\n"
"A1 251,251\n"
"Gvvv 124,220,28\n"
"RC 251,251\n"
"Gvvv 124,216,12\n"
"MU 49,251,49\n"
"A1 224,224\n"
"Gvva 124,212,252\n"
"Tv 220,238,20480\n"
"v_xor_b32 v237, 0, v236\n"
"Gvva 124,208,236\n"
"Tv 222,238,20992\n"
"FS 225,51,92\n"
"Gvva 124,204,220\n"
"Tv 216,238,16384\n"
"MU 225,225,51\n"
"Gvva 124,200,204\n"
"WL 3\n"
"Tv 218,238,16896\n"
"FS 226,52,92\n"
"Gvva 140,176,204\n"
"Tv 212,238,12288\n"
"RC 224,224\n"
"Gvva 140,180,220\n"
"Tv 214,238,12800\n"
"MU 50,224,50\n"
"Gvva 140,184,236\n"
"Tv 208,238,8192\n"
"MU 226,226,52\n"
"Gvva 140,188,252\n"
"Tv 210,238,8704\n"
"FS 227,53,92\n"
"Gvvv 140,192,12\n"
"Tv 204,238,4096\n"
"MU 227,227,53\n"
"Gvvv 140,196,28\n"
"Tv 206,238,4608\n"
"FS 254,54,92\n"
"Gvvv 136,196,24\n"
"Tv 200,238,0\n"
"MU 254,254,54\n"
"Gvvv 136,192,8\n"
"Tv 202,238,512\n"
"v_xor_b32 v238, 24576, v236\n"
"Gvva 136,188,248\n"
"EN 225,225\n"
"Gvva 136,184,232\n"
"EN 226,226\n"
"Gvva 136,180,216\n"
"EN 227,227\n"
"Gvva 136,176,200\n"
"EN 254,254\n"
"Gvva 132,176,196\n"
"A1 225,225\n"
"A1 226,226\n"
"Gvva 132,180,212\n"
"RC 225,225\n"
"Gvva 132,184,228\n"
"RC 226,226\n"
"Gvva 132,188,244\n"
"MU 51,225,51\n"
"MU 52,226,52\n"
"Gvvv 132,192,4\n"
"A1 227,227\n"
"A1 254,254\n"
"FS 251,55,92\n"
"Gvvv 132,196,20\n"
"RC 227,227\n"
"Gvvv 128,196,16\n"
"RC 254,254\n"
"Gvvv 128,192,0\n"
"MU 53,227,53\n"
"MU 54,254,54\n"
"Gvva 128,188,240\n"
"MU 251,251,55\n"
"FS 224,56,92\n"
"Gvva 128,184,224\n"
"EN 251,251\n"
"Gvva 128,180,208\n"
"A1 251,251\n"
"MU 224,224,56\n"
"Gvva 128,176,192\n"
"s_waitcnt vmcnt(8) lgkmcnt(0)\n"
"RC 251,251\n"
"Gvva 144,200,192\n"
"s_barrier\n"
"Tv 196,237,20480\n"
"Gvva 144,204,208\n"
"Tv 198,237,20992\n"
"MU 55,251,55\n"
"Gvva 144,208,224\n"
"Tv 192,237,16384\n"
"FS 225,57,92\n"
"Gvva 144,212,240\n"
"Tv 194,237,16896\n"
"MU 225,225,57\n"
"Gvvv 144,216,0\n"
"Tv 188,237,12288\n"
"FS 226,58,92\n"
"Gvvv 144,220,16\n"
"WV 0\n"
"Tv 190,237,12800\n"
"MU 226,226,58\n"
"Gvvv 148,220,20\n"
"s_add_u32 s58, s58, 4294930432\n"
"buffer_store_dwordx4 v[164:167], v163, s[20:23], s58 offen sc1\n"
"FS 227,59,92\n"
"Gvvv 148,216,4\n"
"Tv 184,237,8192\n"
"MU 227,227,59\n"
"Gvva 148,212,244\n"
"Tv 186,237,8704\n"
"EN 224,224\n"
"Gvva 148,208,228\n"
"Tv 180,237,4096\n"
"A1 224,224\n"
"Gvva 148,204,212\n"
"Tv 182,237,4608\n"
"FS 254,60,92\n"
"Gvva 148,200,196\n"
"s_add_u32 s58, s58, 18432\n"
"buffer_store_dwordx4 v[168:171], v163, s[20:23], s58 offen sc1\n"
"MU 254,254,60\n"
"Gvva 152,200,200\n"
"Tv 176,237,0\n"
"FS 251,61,92\n"
"Gvva 152,204,216\n"
"Tv 178,237,512\n"
"v_xor_b32 v237, 49152, v236\n"
"Gvva 152,208,232\n"
"RC 224,224\n"
"Gvva 152,212,248\n"
"EN 225,225\n"
"Gvvv 152,216,8\n"
"EN 226,226\n"
"Gvvv 152,220,24\n"
"s_add_u32 s58, s58, 4294893568\n"
"buffer_store_dwordx4 v[172:175], v163, s[20:23], s58 offen sc1\n"
"MU 56,224,56\n"
"Gvvv 156,220,28\n"
"EN 227,227\n"
"Gvvv 156,216,12\n"
"EN 254,254\n"
"Gvva 156,212,252\n"
"buffer_load_dword v229, v242, s[28:31], s4 offen offset:128\n"
"A1 225,225\n"
"Gvva 156,208,236\n"
"Tv 220,238,20480\n"
"A1 226,226\n"
"Gvva 156,204,220\n"
"Tv 222,238,20992\n"
"A1 227,227\n"
"Gvva 156,200,204\n"
"WL 2\n"
"Tv 216,238,16384\n"
"A1 254,254\n"
"Gzvva 172,176,12\n"
"Tv 218,238,16896\n"
"RC 225,225\n"
"Gvva 172,180,28\n"
"Tv 212,238,12288\n"
"MU 57,225,57\n"
"Gvva 172,184,44\n"
"Tv 214,238,12800\n"
"MU 251,251,61\n"
"Gvva 172,188,60\n"
"Tv 208,238,8192\n"
"FS 224,62,92\n"
"Gvva 172,192,76\n"
"Tv 210,238,8704\n"
"MU 224,224,62\n"
"Gvva 172,196,92\n"
"Tv 204,238,4096\n"
"FS 225,63,92\n"
"Gzvva 168,196,88\n"
"Tv 206,238,4608\n"
"MU 225,225,63\n"
"Gvva 168,192,72\n"
"Tv 200,238,0\n"
"MU 112,112,48\n"
"Gvva 168,188,56\n"
"Tv 202,238,512\n"
"v_xor_b32 v238, 32, v236\n"
"Gvva 168,184,40\n"
"RC 226,226\n"
"Gvva 168,180,24\n"
"RC 227,227\n"
"Gvva 168,176,8\n"
"RC 254,254\n"
"Gzvva 164,176,4\n"
"EN 251,251\n"
"Gvva 164,180,20\n"
"EN 224,224\n"
"Gvva 164,184,36\n"
"EN 225,225\n"
"Gvva 164,188,52\n"
"MU 58,226,58\n"
"MU 59,227,59\n"
"Gvva 164,192,68\n"
"MU 60,254,60\n"
"A1 251,251\n"
"A1 224,224\n"
"Gvva 164,196,84\n"
"RC 251,251\n"
"Gzvva 160,196,80\n"
"RC 224,224\n"
"Gvva 160,192,64\n"
"MU 61,251,61\n"
"MU 62,224,62\n"
"Gvva 160,188,48\n"
"Tv 196,237,20480\n"
"A1 225,225\n"
"Gvva 160,184,32\n"
"Tv 198,237,20992\n"
"MU 113,113,49\n"
"Gvva 160,180,16\n"
"Tv 192,237,16384\n"
"MU 114,114,50\n"
"Gvva 160,176,0\n"
"WL 3\n"
"Tv 194,237,16896\n"
"MU 115,115,51\n"
"Gvva 160,200,96\n"
"Tv 188,237,12288\n"
"CV 34,112,113\n"
"Gvva 160,204,112\n"
"Tv 190,237,12800\n"
"CV 35,114,115\n"
"Gvva 160,208,128\n"
"buffer_store_dwordx4 v[32:35], v232, s[20:23], s62 offen sc1\n"
"MU 116,116,52\n"
"Gvva 160,212,144\n"
"Tv 184,237,8192\n"
"MU 117,117,53\n"
"Gvva 160,216,160\n"
"Tv 186,237,8704\n"
"MU 118,118,54\n"
"Gvva 160,220,176\n"
"Tv 180,237,4096\n"
"MU 119,119,55\n"
"Gvva 164,220,180\n"
"Tv 182,237,4608\n"
"CV 38,116,117\n"
"Gvva 164,216,164\n"
"Tv 176,237,0\n"
"CV 39,118,119\n"
"Gvva 164,212,148\n"
"buffer_store_dwordx4 v[36:39], v232, s[20:23], s62 offen offset:1024 sc1\n"
"RC 225,225\n"
"Gvva 164,208,132\n"
"Tv 178,237,512\n"
"v_xor_b32 v237, 24608, v236\n"
"Gvva 164,204,116\n"
"MU 63,225,63\n"
"MU 120,120,56\n"
"Gvva 164,200,100\n"
"MU 121,121,57\n"
"MU 122,122,58\n"
"Gvva 168,200,104\n"
"MU 123,123,59\n"
"CV 42,120,121\n"
"Gvva 168,204,120\n"
"CV 43,122,123\n"
"MU 124,124,60\n"
"Gvva 168,208,136\n"
"buffer_store_dwordx4 v[40:43], v232, s[20:23], s62 offen offset:2048 sc1\n"
"MU 125,125,61\n"
"Gvva 168,212,152\n"
"MU 126,126,62\n"
"MU 127,127,63\n"
"Gvva 168,216,168\n"
"CV 46,124,125\n"
"CV 47,126,127\n"
"Gvva 168,220,184\n"
"buffer_store_dwordx4 v[44:47], v232, s[20:23], s62 offen offset:3072 sc1\n"
"FS 226,76,92\n"
"Gvva 172,220,188\n"
"MU 226,226,76\n"
"FS 227,77,92\n"
"Gvva 172,216,172\n"
"EN 226,226\n"
"Gvva 172,212,156\n"
"Tv 220,238,20480\n"
"A1 226,226\n"
"Gvva 172,208,140\n"
"Tv 222,238,20992\n"
"MU 227,227,77\n"
"Gvva 172,204,124\n"
"Tv 216,238,16384\n"
"FS 254,78,92\n"
"Gvva 172,200,108\n"
"WL 3\n"
"Tv 218,238,16896\n"
"MU 254,254,78\n"
"Gvva 172,176,204\n"
"Tv 212,238,12288\n"
"RC 226,226\n"
"Gvva 172,180,220\n"
"Tv 214,238,12800\n"
"MU 76,226,76\n"
"Gvva 172,184,236\n"
"Tv 208,238,8192\n"
"FS 251,79,92\n"
"Gvva 172,188,252\n"
"Tv 210,238,8704\n"
"MU 251,251,79\n"
"Gvvv 172,192,12\n"
"Tv 204,238,4096\n"
"FS 224,72,92\n"
"Gvvv 172,196,28\n"
"Tv 206,238,4608\n"
"MU 224,224,72\n"
"Gvvv 168,196,24\n"
"Tv 200,238,0\n"
"FS 225,73,92\n"
"Gvvv 168,192,8\n"
"Tv 202,238,512\n"
"v_xor_b32 v238, 49184, v236\n"
"Gvva 168,188,248\n"
"EN 227,227\n"
"Gvva 168,184,232\n"
"EN 254,254\n"
"Gvva 168,180,216\n"
"EN 251,251\n"
"Gvva 168,176,200\n"
"EN 224,224\n"
"Gvva 164,176,196\n"
"A1 227,227\n"
"A1 254,254\n"
"Gvva 164,180,212\n"
"RC 227,227\n"
"Gvva 164,184,228\n"
"RC 254,254\n"
"Gvva 164,188,244\n"
"WV 5\n"
"MU 77,227,77\n"
"MU 78,254,78\n"
"Gvvv 164,192,4\n"
"s_barrier\n"
"A1 251,251\n"
"A1 224,224\n"
"Gvvv 164,196,20\n"
"buffer_store_dwordx2 v[230:231], v252, s[20:23], s75 offen sc1\n"
"MU 225,225,73\n"
"Gvvv 160,196,16\n"
"RC 251,251\n"
"Gvvv 160,192,0\n"
"RC 224,224\n"
"Gvva 160,188,240\n"
"Tv 196,237,20480\n"
"MU 79,251,79\n"
"Gvva 160,184,224\n"
"Tv 198,237,20992\n"
"MU 72,224,72\n"
"Gvva 160,180,208\n"
"Tv 192,237,16384\n"
"FS 226,74,92\n"
"Gvva 160,176,192\n"
"WL 3\n"
"Tv 194,237,16896\n"
"MU 226,226,74\n"
"Gzvva 96,200,0\n"
"Tv 188,237,12288\n"
"FS 227,75,92\n"
"Gvva 96,204,16\n"
"Tv 190,237,12800\n"
"MU 227,227,75\n"
"Gvva 96,208,32\n"
"Tv 184,237,8192\n"
"FS 254,68,92\n"
"Gvva 96,212,48\n"
"Tv 186,237,8704\n"
"MU 254,254,68\n"
"Gvva 96,216,64\n"
"Tv 180,237,4096\n"
"FS 251,69,92\n"
"Gvva 96,220,80\n"
"Tv 182,237,4608\n"
"MU 251,251,69\n"
"Gzvva 100,220,84\n"
"Tv 176,237,0\n"
"FS 224,70,92\n"
"Gvva 100,216,68\n"
"Tv 178,237,512\n"
"v_xor_b32 v237, 64, v236\n"
"Gvva 100,212,52\n"
"EN 225,225\n"
"Gvva 100,208,36\n"
"EN 226,226\n"
"Gvva 100,204,20\n"
"EN 227,227\n"
"Gvva 100,200,4\n"
"EN 254,254\n"
"Gzvva 104,200,8\n"
"s_add_u32 m0, s61, 73728\n"
"EN 251,251\n"
"Gvva 104,204,24\n"
"s_add_u32 s53, s11, 128\n"
"L 239,28,53\n"
"A1 225,225\n"
"Gvva 104,208,40\n"
"RC 225,225\n"
"Gvva 104,212,56\n"
"MU 73,225,73\n"
"A1 226,226\n"
"Gvva 104,216,72\n"
"RC 226,226\n"
"Gvva 104,220,88\n"
"MU 74,226,74\n"
"A1 227,227\n"
"Gzvva 108,220,92\n"
"RC 227,227\n"
"Gvva 108,216,76\n"
"s_add_u32 m0, s61, 77824\n"
"MU 75,227,75\n"
"A1 254,254\n"
"Gvva 108,212,60\n"
"s_add_u32 s53, s11, 295040\n"
"L 239,28,53\n"
"A1 251,251\n"
"Gvva 108,208,44\n"
"Tv 220,238,20480\n"
"MU 224,224,70\n"
"Gvva 108,204,28\n"
"Tv 222,238,20992\n"
"FS 225,71,92\n"
"Gvva 108,200,12\n"
"WL 2\n"
"Tv 216,238,16384\n"
"MU 225,225,71\n"
"Gvva 108,176,108\n"
"Tv 218,238,16896\n"
"RC 254,254\n"
"Gvva 108,180,124\n"
"Tv 212,238,12288\n"
"MU 68,254,68\n"
"Gvva 108,184,140\n"
"s_add_u32 m0, s61, 81920\n"
"Tv 214,238,12800\n"
"FS 226,64,92\n"
"Gvva 108,188,156\n"
"s_add_u32 s53, s11, 589952\n"
"L 239,28,53\n"
"MU 226,226,64\n"
"Gvva 108,192,172\n"
"Tv 208,238,8192\n"
"FS 227,65,92\n"
"Gvva 108,196,188\n"
"Tv 210,238,8704\n"
"MU 227,227,65\n"
"Gvva 104,196,184\n"
"Tv 204,238,4096\n"
"FS 254,66,92\n"
"Gvva 104,192,168\n"
"Tv 206,238,4608\n"
"MU 254,254,66\n"
"Gvva 104,188,152\n"
"Tv 200,238,0\n"
"MU 140,140,76\n"
"Gvva 104,184,136\n"
"s_add_u32 m0, s61, 86016\n"
"Tv 202,238,512\n"
"v_xor_b32 v238, 24640, v236\n"
"Gvva 104,180,120\n"
"s_add_u32 s53, s11, 884864\n"
"L 239,28,53\n"
"MU 141,141,77\n"
"Gvva 104,176,104\n"
"RC 251,251\n"
"Gvva 100,176,100\n"
"EN 224,224\n"
"Gvva 100,180,116\n"
"EN 225,225\n"
"Gvva 100,184,132\n"
"EN 226,226\n"
"Gvva 100,188,148\n"
"EN 227,227\n"
"Gvva 100,192,164\n"
"s_add_u32 m0, s61, 90112\n"
"EN 254,254\n"
"Gvva 100,196,180\n"
"s_add_u32 s53, s11, 1179776\n"
"L 239,28,53\n"
"MU 69,251,69\n"
"Gvva 96,196,176\n"
"A1 224,224\n"
"A1 225,225\n"
"Gvva 96,192,160\n"
"RC 224,224\n"
"Gvva 96,188,144\n"
"Tv 196,237,20480\n"
"MU 70,224,70\n"
"Gvva 96,184,128\n"
"Tv 198,237,20992\n"
"A1 226,226\n"
"Gvva 96,180,112\n"
"Tv 192,237,16384\n"
"A1 227,227\n"
"Gvva 96,176,96\n"
"WL 3\n"
"Tv 194,237,16896\n"
"A1 254,254\n"
"Gvva 96,200,192\n"
"s_add_u32 m0, s61, 94208\n"
"Tv 188,237,12288\n"
"FS 251,67,92\n"
"Gvva 96,204,208\n"
"s_add_u32 s53, s11, 1474688\n"
"L 239,28,53\n"
"MU 251,251,67\n"
"Gvva 96,208,224\n"
"Tv 190,237,12800\n"
"MU 142,142,78\n"
"Gvva 96,212,240\n"
"Tv 184,237,8192\n"
"MU 143,143,79\n"
"Gvvv 96,216,0\n"
"Tv 186,237,8704\n"
"CV 76,140,141\n"
"Gvvv 96,220,16\n"
"Tv 180,237,4096\n"
"CV 77,142,143\n"
"Gvvv 100,220,20\n"
"s_add_u32 m0, s61, 98304\n"
"Tv 182,237,4608\n"
"MU 136,136,72\n"
"Gvvv 100,216,4\n"
"s_add_u32 s53, s12, 128\n"
"L 239,28,53\n"
"MU 137,137,73\n"
"Gvva 100,212,244\n"
"Tv 176,237,0\n"
"RC 225,225\n"
"Gvva 100,208,228\n"
"Tv 178,237,512\n"
"v_xor_b32 v237, 49216, v236\n"
"Gvva 100,204,212\n"
"RC 226,226\n"
"Gvva 100,200,196\n"
"RC 227,227\n"
"Gvva 104,200,200\n"
"RC 254,254\n"
"Gvva 104,204,216\n"
"s_add_u32 m0, s61, 102400\n"
"EN 251,251\n"
"Gvva 104,208,232\n"
"s_add_u32 s53, s12, 295040\n"
"L 239,28,53\n"
"MU 71,225,71\n"
"Gvva 104,212,248\n"
"MU 64,226,64\n"
"MU 65,227,65\n"
"Gvvv 104,216,8\n"
"MU 66,254,66\n"
"A1 251,251\n"
"Gvvv 104,220,24\n"
"RC 251,251\n"
"Gvvv 108,220,28\n"
"MU 67,251,67\n"
"MU 138,138,74\n"
"Gvvv 108,216,12\n"
"MU 139,139,75\n"
"CV 72,136,137\n"
"Gvva 108,212,252\n"
"s_add_u32 m0, s61, 106496\n"
"Tv 220,238,20480\n"
"CV 73,138,139\n"
"Gvva 108,208,236\n"
"s_add_u32 s53, s12, 589952\n"
"L 239,28,53\n"
"MU 132,132,68\n"
"Gvva 108,204,220\n"
"Tv 222,238,20992\n"
"MU 133,133,69\n"
"Gvva 108,200,204\n"
"WL 2\n"
"Tv 216,238,16384\n"
"MU 134,134,70\n"
"Gzvva 124,176,12\n"
"Tv 218,238,16896\n"
"MU 135,135,71\n"
"CV 68,132,133\n"
"Gvva 124,180,28\n"
"Tv 212,238,12288\n"
"CV 69,134,135\n"
"Gvva 124,184,44\n"
"Tv 214,238,12800\n"
"MU 128,128,64\n"
"Gvva 124,188,60\n"
"s_add_u32 m0, s61, 110592\n"
"Tv 208,238,8192\n"
"MU 129,129,65\n"
"Gvva 124,192,76\n"
"s_add_u32 s53, s12, 884864\n"
"L 239,28,53\n"
"MU 130,130,66\n"
"Gvva 124,196,92\n"
"Tv 210,238,8704\n"
"MU 131,131,67\n"
"Gzvva 120,196,88\n"
"Tv 204,238,4096\n"
"CV 64,128,129\n"
"Gvva 120,192,72\n"
"Tv 206,238,4608\n"
"CV 65,130,131\n"
"Gvva 120,188,56\n"
"Tv 200,238,0\n"
"FS 224,80,92\n"
"Gvva 120,184,40\n"
"Tv 202,238,512\n"
"v_xor_b32 v238, 96, v236\n"
"Gvva 120,180,24\n"
"s_add_u32 m0, s61, 114688\n"
"MU 224,224,80\n"
"FS 225,81,92\n"
"Gvva 120,176,8\n"
"s_add_u32 s53, s12, 1179776\n"
"L 239,28,53\n"
"MU 225,225,81\n"
"Gzvva 116,176,4\n"
"EN 224,224\n"
"Gvva 116,180,20\n"
"EN 225,225\n"
"Gvva 116,184,36\n"
"A1 224,224\n"
"A1 225,225\n"
"Gvva 116,188,52\n"
"RC 224,224\n"
"Gvva 116,192,68\n"
"RC 225,225\n"
"Gvva 116,196,84\n"
"s_add_u32 m0, s61, 118784\n"
"MU 80,224,80\n"
"MU 81,225,81\n"
"Gzvva 112,196,80\n"
"s_add_u32 s53, s12, 1474688\n"
"L 239,28,53\n"
"FS 226,82,92\n"
"Gvva 112,192,64\n"
"MU 226,226,82\n"
"FS 227,83,92\n"
"Gvva 112,188,48\n"
"Tv 196,237,20480\n"
"MU 227,227,83\n"
"Gvva 112,184,32\n"
"Tv 198,237,20992\n"
"FS 254,84,92\n"
"Gvva 112,180,16\n"
"Tv 192,237,16384\n"
"MU 254,254,84\n"
"Gvva 112,176,0\n"
"WL 3\n"
"Tv 194,237,16896\n"
"FS 251,85,92\n"
"Gvva 112,200,96\n"
"s_add_u32 m0, s61, 122880\n"
"Tv 188,237,12288\n"
"MU 251,251,85\n"
"Gvva 112,204,112\n"
"s_add_u32 s53, s13, 128\n"
"L 239,28,53\n"
"FS 224,86,92\n"
"Gvva 112,208,128\n"
"Tv 190,237,12800\n"
"MU 224,224,86\n"
"Gvva 112,212,144\n"
"Tv 184,237,8192\n"
"FS 225,87,92\n"
"Gvva 112,216,160\n"
"Tv 186,237,8704\n"
"MU 225,225,87\n"
"Gvva 112,220,176\n"
"EN 226,226\n"
"Gvva 116,220,180\n"
"Tv 180,237,4096\n"
"A1 226,226\n"
"Gvva 116,216,164\n"
"s_add_u32 m0, s61, 126976\n"
"RC 226,226\n"
"Gvva 116,212,148\n"
"s_add_u32 s53, s13, 295040\n"
"L 239,28,53\n"
"EN 227,227\n"
"Gvva 116,208,132\n"
"Tv 182,237,4608\n"
"MU 82,226,82\n"
"Gvva 116,204,116\n"
"Tv 176,237,0\n"
"A1 227,227\n"
"Gvva 116,200,100\n"
"Tv 178,237,512\n"
"v_xor_b32 v237, 24672, v236\n"
"Gvva 120,200,104\n"
"RC 227,227\n"
"Gvva 120,204,120\n"
"EN 254,254\n"
"Gvva 120,208,136\n"
"s_add_u32 m0, s61, 131072\n"
"EN 251,251\n"
"Gvva 120,212,152\n"
"s_add_u32 s53, s13, 589952\n"
"L 239,28,53\n"
"MU 83,227,83\n"
"Gvva 120,216,168\n"
"EN 224,224\n"
"Gvva 120,220,184\n"
"EN 225,225\n"
"Gvva 124,220,188\n"
"A1 254,254\n"
"A1 251,251\n"
"Gvva 124,216,172\n"
"RC 254,254\n"
"Gvva 124,212,156\n"
"Tv 220,238,20480\n"
"MU 84,254,84\n"
"Gvva 124,208,140\n"
"s_add_u32 m0, s61, 135168\n"
"Tv 222,238,20992\n"
"A1 224,224\n"
"Gvva 124,204,124\n"
"s_add_u32 s53, s13, 884864\n"
"L 239,28,53\n"
"A1 225,225\n"
"Gvva 124,200,108\n"
"WL 2\n"
"Tv 216,238,16384\n"
"FS 226,88,92\n"
"Gvva 124,176,204\n"
"Tv 218,238,16896\n"
"RC 251,251\n"
"Gvva 124,180,220\n"
"Tv 212,238,12288\n"
"MU 85,251,85\n"
"Gvva 124,184,236\n"
"Tv 214,238,12800\n"
"MU 226,226,88\n"
"Gvva 124,188,252\n"
"Tv 208,238,8192\n"
"FS 227,89,92\n"
"Gvvv 124,192,12\n"
"s_add_u32 m0, s61, 139264\n"
"Tv 210,238,8704\n"
"MU 227,227,89\n"
"Gvvv 124,196,28\n"
"s_add_u32 s53, s13, 1179776\n"
"L 239,28,53\n"
"FS 254,90,92\n"
"Gvvv 120,196,24\n"
"s_add_u32 s62, s62, 4096\n"
"Tv 204,238,4096\n"
"MU 254,254,90\n"
"Gvvv 120,192,8\n"
"Tv 206,238,4608\n"
"FS 251,91,92\n"
"Gvva 120,188,248\n"
"Tv 200,238,0\n"
"MU 251,251,91\n"
"Gvva 120,184,232\n"
"Tv 202,238,512\n"
"v_xor_b32 v238, 49248, v236\n"
"Gvva 120,180,216\n"
"RC 224,224\n"
"Gvva 120,176,200\n"
"s_add_u32 m0, s61, 143360\n"
"RC 225,225\n"
"Gvva 116,176,196\n"
"s_add_u32 s53, s13, 1474688\n"
"L 239,28,53\n"
"MU 86,224,86\n"
"Gvva 116,180,212\n"
"EN 226,226\n"
"Gvva 116,184,228\n"
"EN 227,227\n"
"Gvva 116,188,244\n"
"EN 254,254\n"
"Gvvv 116,192,4\n"
"EN 251,251\n"
"Gvvv 116,196,20\n"
"MU 87,225,87\n"
"A1 226,226\n"
"Gvvv 112,196,16\n"
"RC 226,226\n"
"Gvvv 112,192,0\n"
"MU 88,226,88\n"
"A1 227,227\n"
"Gvva 112,188,240\n"
"Tv 196,237,20480\n"
"A1 254,254\n"
"Gvva 112,184,224\n"
"Tv 198,237,20992\n"
"A1 251,251\n"
"Gvva 112,180,208\n"
"Tv 192,237,16384\n"
"FS 224,92,92\n"
"Gvva 112,176,192\n"
"WL 3\n"
"Tv 194,237,16896\n"
"MU 224,224,92\n"
"Gzvva 128,200,0\n"
"Tv 188,237,12288\n"
"FS 225,93,92\n"
"Gvva 128,204,16\n"
"Tv 190,237,12800\n"
"MU 225,225,93\n"
"Gvva 128,208,32\n"
"Tv 184,237,8192\n"
"FS 226,94,92\n"
"Gvva 128,212,48\n"
"Tv 186,237,8704\n"
"MU 226,226,94\n"
"Gvva 128,216,64\n"
"Tv 180,237,4096\n"
"MU 144,144,80\n"
"Gvva 128,220,80\n"
"Tv 182,237,4608\n"
"MU 145,145,81\n"
"Gzvva 132,220,84\n"
"Tv 176,237,0\n"
"MU 146,146,82\n"
"Gvva 132,216,68\n"
"Tv 178,237,512\n"
"v_xor_b32 v237, 73728, v236\n"
"Gvva 132,212,52\n"
"buffer_load_dwordx2 v[228:229], v241, s[20:23], s93 offen sc1\n"
"RC 227,227\n"
"Gvva 132,208,36\n"
"RC 254,254\n"
"Gvva 132,204,20\n"
"RC 251,251\n"
"Gvva 132,200,4\n"
"EN 224,224\n"
"Gzvva 136,200,8\n"
"EN 225,225\n"
"Gvva 136,204,24\n"
"EN 226,226\n"
"Gvva 136,208,40\n"
"MU 89,227,89\n"
"MU 90,254,90\n"
"Gvva 136,212,56\n"
"MU 91,251,91\n"
"A1 224,224\n"
"Gvva 136,216,72\n"
"RC 224,224\n"
"Gvva 136,220,88\n"
"MU 92,224,92\n"
"A1 225,225\n"
"Gzvva 140,220,92\n"
"RC 225,225\n"
"Gvva 140,216,76\n"
"MU 93,225,93\n"
"A1 226,226\n"
"Gvva 140,212,60\n"
"Tv 220,238,20480\n"
"FS 227,95,92\n"
"Gvva 140,208,44\n"
"Tv 222,238,20992\n"
"MU 227,227,95\n"
"Gvva 140,204,28\n"
"Tv 216,238,16384\n"
"MU 147,147,83\n"
"Gvva 140,200,12\n"
"WL 3\n"
"Tv 218,238,16896\n"
"CV 66,144,145\n"
"Gvva 140,176,108\n"
"Tv 212,238,12288\n"
"RC 226,226\n"
"Gvva 140,180,124\n"
"Tv 214,238,12800\n"
"MU 94,226,94\n"
"Gvva 140,184,140\n"
"Tv 208,238,8192\n"
"CV 67,146,147\n"
"Gvva 140,188,156\n"
"buffer_store_dwordx4 v[64:67], v232, s[20:23], s62 offen sc1\n"
"MU 148,148,84\n"
"Gvva 140,192,172\n"
"Tv 210,238,8704\n"
"MU 149,149,85\n"
"Gvva 140,196,188\n"
"Tv 204,238,4096\n"
"MU 150,150,86\n"
"Gvva 136,196,184\n"
"Tv 206,238,4608\n"
"MU 151,151,87\n"
"Gvva 136,192,168\n"
"Tv 200,238,0\n"
"CV 70,148,149\n"
"Gvva 136,188,152\n"
"Tv 202,238,512\n"
"v_xor_b32 v238, 98304, v236\n"
"Gvva 136,184,136\n"
"EN 227,227\n"
"Gvva 136,180,120\n"
"A1 227,227\n"
"CV 71,150,151\n"
"Gvva 136,176,104\n"
"buffer_store_dwordx4 v[68:71], v232, s[20:23], s62 offen offset:1024 sc1\n"
"MU 152,152,88\n"
"Gvva 132,176,100\n"
"RC 227,227\n"
"Gvva 132,180,116\n"
"MU 95,227,95\n"
"MU 153,153,89\n"
"Gvva 132,184,132\n"
"MU 154,154,90\n"
"MU 155,155,91\n"
"Gvva 132,188,148\n"
"CV 74,152,153\n"
"CV 75,154,155\n"
"Gvva 132,192,164\n"
"buffer_store_dwordx4 v[72:75], v232, s[20:23], s62 offen offset:2048 sc1\n"
"MU 156,156,92\n"
"MU 157,157,93\n"
"Gvva 132,196,180\n"
"MU 158,158,94\n"
"MU 159,159,95\n"
"Gvva 128,196,176\n"
"CV 78,156,157\n"
"CV 79,158,159\n"
"Gvva 128,192,160\n"
"buffer_store_dwordx4 v[76:79], v232, s[20:23], s62 offen offset:3072 sc1\n"
"FS 254,172,92\n"
"Gvva 128,188,144\n"
"MU 254,254,172\n"
"FS 251,173,92\n"
"Gvva 128,184,128\n"
"EN 254,254\n"
"Gvva 128,180,112\n"
"A1 254,254\n"
"MU 251,251,173\n"
"Gvva 128,176,96\n"
"s_waitcnt vmcnt(5) lgkmcnt(0)\n"
"RC 254,254\n"
"Gvva 128,200,192\n"
"s_barrier\n"
"Tv 196,237,20480\n"
"Gvva 128,204,208\n"
"Tv 198,237,20992\n"
"MU 172,254,172\n"
"Gvva 128,208,224\n"
"Tv 192,237,16384\n"
"FS 224,174,92\n"
"Gvva 128,212,240\n"
"Tv 194,237,16896\n"
"MU 224,224,174\n"
"Gvvv 128,216,0\n"
"Tv 188,237,12288\n"
"FS 225,175,92\n"
"Gvvv 128,220,16\n"
"Tv 190,237,12800\n"
"MU 225,225,175\n"
"Gvvv 132,220,20\n"
"Tv 184,237,8192\n"
"FS 226,168,92\n"
"Gvvv 132,216,4\n"
"Tv 186,237,8704\n"
"MU 226,226,168\n"
"Gvva 132,212,244\n"
"Tv 180,237,4096\n"
"EN 251,251\n"
"Gvva 132,208,228\n"
"Tv 182,237,4608\n"
"A1 251,251\n"
"Gvva 132,204,212\n"
"Tv 176,237,0\n"
"FS 227,169,92\n"
"Gvva 132,200,196\n"
"Tv 178,237,512\n"
"v_xor_b32 v237, 122880, v236\n"
"Gvva 136,200,200\n"
"RC 251,251\n"
"Gvva 136,204,216\n"
"EN 224,224\n"
"Gvva 136,208,232\n"
"EN 225,225\n"
"Gvva 136,212,248\n"
"EN 226,226\n"
"Gvvv 136,216,8\n"
"WV 4\n"
"v_cmp_ne_u64 vcc, s[82:83], v[228:229]\n"
"s_cbranch_vccnz SLS%=\n"
"BKS%=:\n"
"Gvvv 136,220,24\n"
"MU 173,251,173\n"
"A1 224,224\n"
"Gvvv 140,220,28\n"
"RC 224,224\n"
"Gvvv 140,216,12\n"
"MU 174,224,174\n"
"A1 225,225\n"
"Gvva 140,212,252\n"
"Tv 220,238,20480\n"
"A1 226,226\n"
"Gvva 140,208,236\n"
"Tv 222,238,20992\n"
"MU 227,227,169\n"
"Gvva 140,204,220\n"
"Tv 216,238,16384\n"
"FS 254,170,92\n"
"Gvva 140,200,204\n"
"WL 3\n"
"Tv 218,238,16896\n"
"MU 254,254,170\n"
"Gzvva 156,176,12\n"
"Tv 212,238,12288\n"
"FS 251,171,92\n"
"Gvva 156,180,28\n"
"Tv 214,238,12800\n"
"MU 251,251,171\n"
"Gvva 156,184,44\n"
"Tv 208,238,8192\n"
"FS 224,164,92\n"
"Gvva 156,188,60\n"
"Tv 210,238,8704\n"
"MU 224,224,164\n"
"Gvva 156,192,76\n"
"RC 225,225\n"
"Gvva 156,196,92\n"
"Tv 204,238,4096\n"
"MU 175,225,175\n"
"Gzvva 152,196,88\n"
"buffer_load_dwordx4 v[32:35], v232, s[20:23], s59 offen sc1\n"
"FS 225,165,92\n"
"Gvva 152,192,72\n"
"Tv 206,238,4608\n"
"MU 225,225,165\n"
"Gvva 152,188,56\n"
"RC 226,226\n"
"Gvva 152,184,40\n"
"Tv 200,238,0\n"
"MU 168,226,168\n"
"Gvva 152,180,24\n"
"Tv 202,238,512\n"
"v_xor_b32 v238, 73760, v236\n"
"Gvva 152,176,8\n"
"EN 227,227\n"
"Gzvva 148,176,4\n"
"EN 254,254\n"
"Gvva 148,180,20\n"
"EN 251,251\n"
"Gvva 148,184,36\n"
"EN 224,224\n"
"Gvva 148,188,52\n"
"s_add_u32 s59, s59, 4096\n"
"buffer_load_dwordx4 v[36:39], v232, s[20:23], s59 offen sc1\n"
"A1 227,227\n"
"Gvva 148,192,68\n"
"RC 227,227\n"
"Gvva 148,196,84\n"
"EN 225,225\n"
"Gzvva 144,196,80\n"
"MU 169,227,169\n"
"A1 254,254\n"
"Gvva 144,192,64\n"
"RC 254,254\n"
"Gvva 144,188,48\n"
"Tv 196,237,20480\n"
"MU 170,254,170\n"
"Gvva 144,184,32\n"
"Tv 198,237,20992\n"
"A1 251,251\n"
"Gvva 144,180,16\n"
"Tv 192,237,16384\n"
"A1 224,224\n"
"Gvva 144,176,0\n"
"WL 3\n"
"Tv 194,237,16896\n"
"A1 225,225\n"
"Gvva 144,200,96\n"
"s_add_u32 s59, s59, 4096\n"
"buffer_load_dwordx4 v[40:43], v232, s[20:23], s59 offen sc1\n"
"FS 226,166,92\n"
"Gvva 144,204,112\n"
"Tv 188,237,12288\n"
"MU 226,226,166\n"
"Gvva 144,208,128\n"
"Tv 190,237,12800\n"
"FS 227,167,92\n"
"Gvva 144,212,144\n"
"Tv 184,237,8192\n"
"MU 227,227,167\n"
"Gvva 144,216,160\n"
"Tv 186,237,8704\n"
"FS 254,160,92\n"
"Gvva 144,220,176\n"
"Tv 180,237,4096\n"
"MU 254,254,160\n"
"Gvva 148,220,180\n"
"RC 251,251\n"
"Gvva 148,216,164\n"
"Tv 182,237,4608\n"
"MU 171,251,171\n"
"Gvva 148,212,148\n"
"Tv 176,237,0\n"
"FS 251,161,92\n"
"Gvva 148,208,132\n"
"s_add_u32 s59, s59, 4096\n"
"buffer_load_dwordx4 v[44:47], v232, s[20:23], s59 offen sc1\n"
"MU 251,251,161\n"
"Gvva 148,204,116\n"
"RC 224,224\n"
"Gvva 148,200,100\n"
"Tv 178,237,512\n"
"v_xor_b32 v237, 98336, v236\n"
"Gvva 152,200,104\n"
"RC 225,225\n"
"Gvva 152,204,120\n"
"EN 226,226\n"
"Gvva 152,208,136\n"
"EN 227,227\n"
"Gvva 152,212,152\n"
"EN 254,254\n"
"Gvva 152,216,168\n"
"EN 251,251\n"
"Gvva 152,220,184\n"
"MU 164,224,164\n"
"MU 165,225,165\n"
"Gvva 156,220,188\n"
"s_add_u32 s59, s59, 4096\n"
"buffer_load_dwordx4 v[48:51], v232, s[20:23], s59 offen sc1\n"
"A1 226,226\n"
"Gvva 156,216,172\n"
"RC 226,226\n"
"Gvva 156,212,156\n"
"Tv 220,238,20480\n"
"MU 166,226,166\n"
"Gvva 156,208,140\n"
"Tv 222,238,20992\n"
"A1 227,227\n"
"Gvva 156,204,124\n"
"Tv 216,238,16384\n"
"A1 254,254\n"
"Gvva 156,200,108\n"
"s_waitcnt vmcnt(5) lgkmcnt(3)\n"
"buffer_store_dwordx2 v[230:231], v252, s[20:23], s76 offen sc1\n"
"A1 251,251\n"
"Gvva 156,176,204\n"
"Tv 218,238,16896\n"
"FS 224,162,92\n"
"Gvva 156,180,220\n"
"Tv 212,238,12288\n"
"MU 224,224,162\n"
"Gvva 156,184,236\n"
"Tv 214,238,12800\n"
"FS 225,163,92\n"
"Gvva 156,188,252\n"
"s_add_u32 s59, s59, 4096\n"
"buffer_load_dwordx4 v[52:55], v232, s[20:23], s59 offen sc1\n"
"MU 225,225,163\n"
"Gvvv 156,192,12\n"
"Tv 208,238,8192\n"
"FS 226,96,92\n"
"Gvvv 156,196,28\n"
"Tv 210,238,8704\n"
"MU 226,226,96\n"
"Gvvv 152,196,24\n"
"RC 227,227\n"
"Gvvv 152,192,8\n"
"Tv 204,238,4096\n"
"MU 167,227,167\n"
"Gvva 152,188,248\n"
"Tv 206,238,4608\n"
"MU 156,156,172\n"
"Gvva 152,184,232\n"
"Tv 200,238,0\n"
"MU 157,157,173\n"
"Gvva 152,180,216\n"
"Tv 202,238,512\n"
"v_xor_b32 v238, 122912, v236\n"
"Gvva 152,176,200\n"
"RC 254,254\n"
"Gvva 148,176,196\n"
"s_add_u32 s59, s59, 4096\n"
"buffer_load_dwordx4 v[56:59], v232, s[20:23], s59 offen sc1\n"
"MU 160,254,160\n"
"Gvva 148,180,212\n"
"RC 251,251\n"
"Gvva 148,184,228\n"
"EN 224,224\n"
"Gvva 148,188,244\n"
"EN 225,225\n"
"Gvvv 148,192,4\n"
"EN 226,226\n"
"Gvvv 148,196,20\n"
"MU 161,251,161\n"
"A1 224,224\n"
"Gvvv 144,196,16\n"
"RC 224,224\n"
"Gvvv 144,192,0\n"
"MU 162,224,162\n"
"A1 225,225\n"
"Gvva 144,188,240\n"
"Tv 196,237,20480\n"
"MU 158,158,174\n"
"Gvva 144,184,224\n"
"s_add_u32 s59, s59, 4096\n"
"buffer_load_dwordx4 v[60:63], v232, s[20:23], s59 offen sc1\n"
"MU 159,159,175\n"
"Gvva 144,180,208\n"
"WV 8\n"
"ds_write_b128 v246, v[32:35] offset:0\n"
"CV 172,156,157\n"
"Gvva 144,176,192\n"
"WL 2\n"
"Tv 198,237,20992\n"
"CV 173,158,159\n"
"Gzvva 64,200,0\n"
"Tv 192,237,16384\n"
"MU 152,152,168\n"
"Gvva 64,204,16\n"
"Tv 194,237,16896\n"
"MU 153,153,169\n"
"Gvva 64,208,32\n"
"Tv 188,237,12288\n"
"MU 154,154,170\n"
"Gvva 64,212,48\n"
"Tv 190,237,12800\n"
"MU 155,155,171\n"
"Gvva 64,216,64\n"
"Tv 184,237,8192\n"
"CV 168,152,153\n"
"Gvva 64,220,80\n"
"Tv 186,237,8704\n"
"CV 169,154,155\n"
"Gzvva 68,220,84\n"
"s_add_u32 s59, s59, 4096\n"
"buffer_load_dwordx4 v[32:35], v232, s[20:23], s59 offen sc1\n"
"MU 148,148,164\n"
"Gvva 68,216,68\n"
"WV 8\n"
"ds_write_b128 v246, v[36:39] offset:4096\n"
"MU 149,149,165\n"
"Gvva 68,212,52\n"
"Tv 180,237,4096\n"
"MU 150,150,166\n"
"Gvva 68,208,36\n"
"Tv 182,237,4608\n"
"MU 151,151,167\n"
"Gvva 68,204,20\n"
"Tv 176,237,0\n"
"CV 164,148,149\n"
"Gvva 68,200,4\n"
"Tv 178,237,512\n"
"v_xor_b32 v237, 73792, v236\n"
"Gzvva 72,200,8\n"
"RC 225,225\n"
"Gvva 72,204,24\n"
"MU 163,225,163\n"
"CV 165,150,151\n"
"Gvva 72,208,40\n"
"MU 144,144,160\n"
"MU 145,145,161\n"
"Gvva 72,212,56\n"
"s_add_u32 s59, s59, 4096\n"
"buffer_load_dwordx4 v[36:39], v232, s[20:23], s59 offen sc1\n"
"MU 146,146,162\n"
"Gvva 72,216,72\n"
"WV 8\n"
"ds_write_b128 v246, v[40:43] offset:8192\n"
"MU 147,147,163\n"
"Gvva 72,220,88\n"
"CV 160,144,145\n"
"CV 161,146,147\n"
"Gzvva 76,220,92\n"
"A1 226,226\n"
"FS 227,97,92\n"
"Gvva 76,216,76\n"
"RC 226,226\n"
"Gvva 76,212,60\n"
"Tv 220,238,20480\n"
"MU 96,226,96\n"
"Gvva 76,208,44\n"
"Tv 222,238,20992\n"
"MU 227,227,97\n"
"Gvva 76,204,28\n"
"Tv 216,238,16384\n"
"FS 254,98,92\n"
"Gvva 76,200,12\n"
"WL 4\n"
"Tv 218,238,16896\n"
"MU 254,254,98\n"
"Gvva 76,176,108\n"
"s_add_u32 s59, s59, 4096\n"
"buffer_load_dwordx4 v[40:43], v232, s[20:23], s59 offen sc1\n"
"FS 251,99,92\n"
"Gvva 76,180,124\n"
"WV 8\n"
"ds_write_b128 v246, v[44:47] offset:12288\n"
"MU 251,251,99\n"
"Gvva 76,184,140\n"
"Tv 212,238,12288\n"
"FS 224,100,92\n"
"Gvva 76,188,156\n"
"Tv 214,238,12800\n"
"MU 224,224,100\n"
"Gvva 76,192,172\n"
"Tv 208,238,8192\n"
"FS 225,101,92\n"
"Gvva 76,196,188\n"
"Tv 210,238,8704\n"
"MU 225,225,101\n"
"Gvva 72,196,184\n"
"Tv 204,238,4096\n"
"FS 226,102,92\n"
"Gvva 72,192,168\n"
"Tv 206,238,4608\n"
"MU 226,226,102\n"
"Gvva 72,188,152\n"
"EN 227,227\n"
"Gvva 72,184,136\n"
"s_add_u32 s59, s59, 4096\n"
"buffer_load_dwordx4 v[44:47], v232, s[20:23], s59 offen sc1\n"
"A1 227,227\n"
"Gvva 72,180,120\n"
"WV 8\n"
"ds_write_b128 v246, v[48:51] offset:16384\n"
"Gvva 72,176,104\n"
"RC 227,227\n"
"Gvva 68,176,100\n"
"Tv 200,238,0\n"
"MU 97,227,97\n"
"Gvva 68,180,116\n"
"Tv 202,238,512\n"
"v_xor_b32 v238, 98368, v236\n"
"Gvva 68,184,132\n"
"EN 254,254\n"
"Gvva 68,188,148\n"
"EN 251,251\n"
"Gvva 68,192,164\n"
"EN 224,224\n"
"Gvva 68,196,180\n"
"EN 225,225\n"
"Gvva 64,196,176\n"
"s_add_u32 s59, s59, 4096\n"
"buffer_load_dwordx4 v[48:51], v232, s[20:23], s59 offen sc1\n"
"A1 254,254\n"
"Gvva 64,192,160\n"
"WV 7\n"
"ds_write_b128 v246, v[52:55] offset:20480\n"
"A1 251,251\n"
"Gvva 64,188,144\n"
"Tv 196,237,20480\n"
"A1 224,224\n"
"Gvva 64,184,128\n"
"Tv 198,237,20992\n"
"A1 225,225\n"
"Gvva 64,180,112\n"
"Tv 192,237,16384\n"
"FS 227,103,92\n"
"Gvva 64,176,96\n"
"WL 4\n"
"Tv 194,237,16896\n"
"MU 227,227,103\n"
"Gvva 64,200,192\n"
"RC 254,254\n"
"Gvva 64,204,208\n"
"Tv 188,237,12288\n"
"MU 98,254,98\n"
"Gvva 64,208,224\n"
"Tv 190,237,12800\n"
"FS 254,104,92\n"
"Gvva 64,212,240\n"
"s_add_u32 s59, s59, 4096\n"
"buffer_load_dwordx4 v[52:55], v232, s[20:23], s59 offen sc1\n"
"MU 254,254,104\n"
"Gvvv 64,216,0\n"
"WV 7\n"
"ds_write_b128 v246, v[56:59] offset:24576\n"
"Gvvv 64,220,16\n"
"RC 251,251\n"
"Gvvv 68,220,20\n"
"Tv 184,237,8192\n"
"MU 99,251,99\n"
"Gvvv 68,216,4\n"
"Tv 186,237,8704\n"
"FS 251,105,92\n"
"Gvva 68,212,244\n"
"Tv 180,237,4096\n"
"MU 251,251,105\n"
"Gvva 68,208,228\n"
"RC 224,224\n"
"Gvva 68,204,212\n"
"Tv 182,237,4608\n"
"MU 100,224,100\n"
"Gvva 68,200,196\n"
"Tv 176,237,0\n"
"FS 224,106,92\n"
"Gvva 72,200,200\n"
"s_add_u32 s59, s59, 4096\n"
"buffer_load_dwordx4 v[56:59], v232, s[20:23], s59 offen sc1\n"
"MU 224,224,106\n"
"Gvva 72,204,216\n"
"WV 7\n"
"ds_write_b128 v246, v[60:63] offset:28672\n"
"Gvva 72,208,232\n"
"RC 225,225\n"
"Gvva 72,212,248\n"
"Tv 178,237,512\n"
"v_xor_b32 v237, 122944, v236\n"
"Gvvv 72,216,8\n"
"EN 226,226\n"
"Gvvv 72,220,24\n"
"EN 227,227\n"
"Gvvv 76,220,28\n"
"EN 254,254\n"
"Gvvv 76,216,12\n"
"s_add_u32 s62, s62, 28672\n"
"EN 251,251\n"
"Gvva 76,212,252\n"
"Tv 220,238,20480\n"
"MU 101,225,101\n"
"Gvva 76,208,236\n"
"s_add_u32 s59, s59, 4096\n"
"buffer_load_dwordx4 v[60:63], v232, s[20:23], s59 offen sc1\n"
"A1 226,226\n"
"Gvva 76,204,220\n"
"WV 7\n"
"ds_write_b128 v246, v[32:35] offset:32768\n"
"A1 227,227\n"
"Gvva 76,200,204\n"
"WL 2\n"
"Tv 222,238,20992\n"
"A1 254,254\n"
"Gzvva 92,176,12\n"
"Tv 216,238,16384\n"
"A1 251,251\n"
"Gvva 92,180,28\n"
"Tv 218,238,16896\n"
"FS 225,107,92\n"
"Gvva 92,184,44\n"
"Tv 212,238,12288\n"
"MU 225,225,107\n"
"Gvva 92,188,60\n"
"Tv 214,238,12800\n"
"MU 64,64,96\n"
"Gvva 92,192,76\n"
"Tv 208,238,8192\n"
"MU 65,65,97\n"
"Gvva 92,196,92\n"
"Tv 210,238,8704\n"
"MU 66,66,98\n"
"Gzvva 88,196,88\n"
"s_add_u32 s59, s59, 4096\n"
"buffer_load_dwordx4 v[32:35], v232, s[20:23], s59 offen sc1\n"
"MU 67,67,99\n"
"Gvva 88,192,72\n"
"WV 7\n"
"ds_write_b128 v246, v[36:39] offset:36864\n"
"CV 162,64,65\n"
"Gvva 88,188,56\n"
"Tv 204,238,4096\n"
"CV 163,66,67\n"
"Gvva 88,184,40\n"
"buffer_store_dwordx4 v[160:163], v232, s[20:23], s62 offen sc1\n"
"MU 68,68,100\n"
"Gvva 88,180,24\n"
"Tv 206,238,4608\n"
"MU 69,69,101\n"
"Gvva 88,176,8\n"
"Tv 200,238,0\n"
"CV 166,68,69\n"
"Gzvva 84,176,4\n"
"RC 226,226\n"
"Gvva 84,180,20\n"
"Tv 202,238,512\n"
"v_xor_b32 v238, 73824, v236\n"
"Gvva 84,184,36\n"
"RC 227,227\n"
"Gvva 84,188,52\n"
"s_add_u32 s59, s59, 4096\n"
"buffer_load_dwordx4 v[36:39], v232, s[20:23], s59 offen sc1\n"
"MU 102,226,102\n"
"Gvva 84,192,68\n"
"WV 8\n"
"ds_write_b128 v246, v[40:43] offset:40960\n"
"MU 103,227,103\n"
"Gvva 84,196,84\n"
"RC 254,254\n"
"Gzvva 80,196,80\n"
"RC 251,251\n"
"Gvva 80,192,64\n"
"EN 224,224\n"
"Gvva 80,188,48\n"
"Tv 196,237,20480\n"
"MU 104,254,104\n"
"Gvva 80,184,32\n"
"Tv 198,237,20992\n"
"MU 105,251,105\n"
"Gvva 80,180,16\n"
"buffer_load_dword v229, v243, s[36:39], s5 offen\n"
"A1 224,224\n"
"Gvva 80,176,0\n"
"WL 3\n"
"Tv 192,237,16384\n"
"FS 226,108,92\n"
"Gvva 80,200,96\n"
"Tv 194,237,16896\n"
"MU 226,226,108\n"
"Gvva 80,204,112\n"
"WV 8\n"
"ds_write_b128 v246, v[44:47] offset:45056\n"
"FS 227,109,92\n"
"Gvva 80,208,128\n"
"Tv 188,237,12288\n"
"MU 227,227,109\n"
"Gvva 80,212,144\n"
"Tv 190,237,12800\n"
"FS 254,110,92\n"
"Gvva 80,216,160\n"
"Tv 184,237,8192\n"
"MU 254,254,110\n"
"Gvva 80,220,176\n"
"Tv 186,237,8704\n"
"FS 251,111,92\n"
"Gvva 84,220,180\n"
"Tv 180,237,4096\n"
"MU 251,251,111\n"
"Gvva 84,216,164\n"
"Tv 182,237,4608\n"
"MU 70,70,102\n"
"Gvva 84,212,148\n"
"Tv 176,237,0\n"
"MU 71,71,103\n"
"Gvva 84,208,132\n"
"Tv 178,237,512\n"
"v_xor_b32 v237, 98400, v236\n"
"Gvva 84,204,116\n"
"WV 7\n"
"ds_write_b128 v246, v[48:51] offset:49152\n"
"CV 167,70,71\n"
"Gvva 84,200,100\n"
"buffer_store_dwordx4 v[164:167], v232, s[20:23], s62 offen offset:1024 sc1\n"
"MU 72,72,104\n"
"Gvva 88,200,104\n"
"RC 224,224\n"
"Gvva 88,204,120\n"
"EN 225,225\n"
"Gvva 88,208,136\n"
"EN 226,226\n"
"Gvva 88,212,152\n"
"EN 227,227\n"
"Gvva 88,216,168\n"
"EN 254,254\n"
"Gvva 88,220,184\n"
"EN 251,251\n"
"Gvva 92,220,188\n"
"MU 106,224,106\n"
"A1 225,225\n"
"Gvva 92,216,172\n"
"WV 7\n"
"ds_write_b128 v246, v[52:55] offset:53248\n"
"A1 226,226\n"
"Gvva 92,212,156\n"
"Tv 220,238,20480\n"
"A1 227,227\n"
"Gvva 92,208,140\n"
"Tv 222,238,20992\n"
"A1 254,254\n"
"Gvva 92,204,124\n"
"Tv 216,238,16384\n"
"A1 251,251\n"
"Gvva 92,200,108\n"
"WL 5\n"
"Tv 218,238,16896\n"
"MU 73,73,105\n"
"Gvva 92,176,204\n"
"Tv 212,238,12288\n"
"MU 74,74,106\n"
"Gvva 92,180,220\n"
"Tv 214,238,12800\n"
"CV 170,72,73\n"
"Gvva 92,184,236\n"
"Tv 208,238,8192\n"
"FS 224,124,92\n"
"Gvva 92,188,252\n"
"Tv 210,238,8704\n"
"MU 224,224,124\n"
"Gvvv 92,192,12\n"
"WV 6\n"
"ds_write_b128 v246, v[56:59] offset:57344\n"
"Gvvv 92,196,28\n"
"RC 225,225\n"
"Gvvv 88,196,24\n"
"Tv 204,238,4096\n"
"MU 107,225,107\n"
"Gvvv 88,192,8\n"
"Tv 206,238,4608\n"
"MU 75,75,107\n"
"Gvva 88,188,248\n"
"Tv 200,238,0\n"
"CV 171,74,75\n"
"Gvva 88,184,232\n"
"buffer_store_dwordx4 v[168:171], v232, s[20:23], s62 offen offset:2048 sc1\n"
"FS 225,125,92\n"
"Gvva 88,180,216\n"
"Tv 202,238,512\n"
"v_xor_b32 v238, 122976, v236\n"
"Gvva 88,176,200\n"
"RC 226,226\n"
"Gvva 84,176,196\n"
"RC 227,227\n"
"Gvva 84,180,212\n"
"WV 6\n"
"ds_write_b128 v246, v[60:63] offset:61440\n"
"MU 108,226,108\n"
"Gvva 84,184,228\n"
"RC 254,254\n"
"Gvva 84,188,244\n"
"RC 251,251\n"
"Gvvv 84,192,4\n"
"EN 224,224\n"
"Gvvv 84,196,20\n"
"MU 109,227,109\n"
"MU 110,254,110\n"
"Gvvv 80,196,16\n"
"MU 111,251,111\n"
"MU 76,76,108\n"
"Gvvv 80,192,0\n"
"MU 77,77,109\n"
"MU 78,78,110\n"
"Gvva 80,188,240\n"
"Tv 196,237,20480\n"
"MU 79,79,111\n"
"Gvva 80,184,224\n"
"Tv 198,237,20992\n"
"CV 174,76,77\n"
"Gvva 80,180,208\n"
"WV 5\n"
"ds_write_b128 v247, v[32:35] offset:0\n"
"CV 175,78,79\n"
"Gvva 80,176,192\n"
"WL 4\n"
"buffer_store_dwordx4 v[172:175], v232, s[20:23], s62 offen offset:3072 sc1\n"
"A1 224,224\n"
"Gzvva 144,200,0\n"
"Tv 192,237,16384\n"
"MU 225,225,125\n"
"Gvva 144,204,16\n"
"Tv 194,237,16896\n"
"FS 226,126,92\n"
"Gvva 144,208,32\n"
"Tv 188,237,12288\n"
"MU 226,226,126\n"
"Gvva 144,212,48\n"
"Tv 190,237,12800\n"
"FS 227,127,92\n"
"Gvva 144,216,64\n"
"Tv 184,237,8192\n"
"MU 227,227,127\n"
"Gvva 144,220,80\n"
"Tv 186,237,8704\n"
"FS 254,120,92\n"
"Gzvva 148,220,84\n"
"Tv 180,237,4096\n"
"MU 254,254,120\n"
"Gvva 148,216,68\n"
"WV 4\n"
"ds_write_b128 v247, v[36:39] offset:4096\n"
"FS 251,121,92\n"
"Gvva 148,212,52\n"
"Tv 182,237,4608\n"
"MU 251,251,121\n"
"Gvva 148,208,36\n"
"RC 224,224\n"
"Gvva 148,204,20\n"
"Tv 176,237,0\n"
"MU 124,224,124\n"
"Gvva 148,200,4\n"
"Tv 178,237,512\n"
"FS 224,122,92\n"
"Gzvva 152,200,8\n"
"EN 225,225\n"
"Gvva 152,204,24\n"
"EN 226,226\n"
"Gvva 152,208,40\n"
"EN 227,227\n"
"Gvva 152,212,56\n"
"EN 254,254\n"
"Gvva 152,216,72\n"
"EN 251,251\n"
"Gvva 152,220,88\n"
"A1 225,225\n"
"A1 226,226\n"
"Gzvva 156,220,92\n"
"RC 225,225\n"
"Gvva 156,216,76\n"
"RC 226,226\n"
"Gvva 156,212,60\n"
"Tv 220,238,20480\n"
"MU 125,225,125\n"
"Gvva 156,208,44\n"
"Tv 222,238,20992\n"
"MU 126,226,126\n"
"Gvva 156,204,28\n"
"Tv 216,238,16384\n"
"A1 227,227\n"
"Gvva 156,200,12\n"
"WL 3\n"
"Tv 218,238,16896\n"
"A1 254,254\n"
"Gvva 156,176,108\n"
"Tv 212,238,12288\n"
"A1 251,251\n"
"Gvva 156,180,124\n"
"Tv 214,238,12800\n"
"MU 224,224,122\n"
"Gvva 156,184,140\n"
"Tv 208,238,8192\n"
"FS 225,123,92\n"
"Gvva 156,188,156\n"
"Tv 210,238,8704\n"
"MU 225,225,123\n"
"Gvva 156,192,172\n"
"Tv 204,238,4096\n"
"FS 226,116,92\n"
"Gvva 156,196,188\n"
"Tv 206,238,4608\n"
"MU 226,226,116\n"
"Gvva 152,196,184\n"
"RC 227,227\n"
"Gvva 152,192,168\n"
"Tv 200,238,0\n"
"MU 127,227,127\n"
"Gvva 152,188,152\n"
"Tv 202,238,512\n"
"FS 227,117,92\n"
"Gvva 152,184,136\n"
"RC 254,254\n"
"Gvva 152,180,120\n"
"RC 251,251\n"
"Gvva 152,176,104\n"
"EN 224,224\n"
"Gvva 148,176,100\n"
"EN 225,225\n"
"Gvva 148,180,116\n"
"EN 226,226\n"
"Gvva 148,184,132\n"
"MU 120,254,120\n"
"MU 121,251,121\n"
"Gvva 148,188,148\n"
"A1 224,224\n"
"A1 225,225\n"
"Gvva 148,192,164\n"
"RC 224,224\n"
"Gvva 148,196,180\n"
"RC 225,225\n"
"Gvva 144,196,176\n"
"MU 122,224,122\n"
"MU 123,225,123\n"
"Gvva 144,192,160\n"
"A1 226,226\n"
"MU 227,227,117\n"
"Gvva 144,188,144\n"
"RC 226,226\n"
"Gvva 144,184,128\n"
"EN 227,227\n"
"Gvva 144,180,112\n"
"MU 116,226,116\n"
"A1 227,227\n"
"Gvva 144,176,96\n"
"WL 0\n"
"RC 227,227\n"
"Gvva 144,200,192\n"
"s_barrier\n"
"v_add_u32 v237, 55296, v233\n"
"Gvva 144,204,208\n"
"Tv 196,233,5120\n"
"MU 117,227,117\n"
"Gvva 144,208,224\n"
"s_add_u32 s59, s59, 4096\n"
"buffer_load_dwordx4 v[40:43], v232, s[20:23], s59 offen sc1\n"
"FS 254,118,92\n"
"Gvva 144,212,240\n"
"Tv 198,233,5632\n"
"MU 254,254,118\n"
"Gvvv 144,216,0\n"
"Tv 192,233,4096\n"
"FS 251,119,92\n"
"Gvvv 144,220,16\n"
"Tv 194,233,4608\n"
"MU 251,251,119\n"
"Gvvv 148,220,20\n"
"Tv 188,233,3072\n"
"FS 224,112,92\n"
"Gvvv 148,216,4\n"
"Tv 190,233,3584\n"
"MU 224,224,112\n"
"Gvva 148,212,244\n"
"Tv 184,233,2048\n"
"FS 225,113,92\n"
"Gvva 148,208,228\n"
"Tv 186,233,2560\n"
"MU 225,225,113\n"
"Gvva 148,204,212\n"
"Tv 180,233,1024\n"
"FS 226,114,92\n"
"Gvva 148,200,196\n"
"s_add_u32 s59, s59, 4096\n"
"buffer_load_dwordx4 v[44:47], v232, s[20:23], s59 offen sc1\n"
"MU 226,226,114\n"
"Gvva 152,200,200\n"
"Tv 182,233,1536\n"
"FS 227,115,92\n"
"Gvva 152,204,216\n"
"Tv 176,233,0\n"
"MU 227,227,115\n"
"Gvva 152,208,232\n"
"Tv 178,233,512\n"
"MU 92,92,124\n"
"Gvva 152,212,248\n"
"EN 254,254\n"
"Gvvv 152,216,8\n"
"EN 251,251\n"
"Gvvv 152,220,24\n"
"EN 224,224\n"
"Gvvv 156,220,28\n"
"EN 225,225\n"
"Gvvv 156,216,12\n"
"EN 226,226\n"
"Gvva 156,212,252\n"
"s_add_u32 s59, s59, 4096\n"
"buffer_load_dwordx4 v[48:51], v232, s[20:23], s59 offen sc1\n"
"A1 254,254\n"
"Gvva 156,208,236\n"
"Tv 220,233,11264\n"
"A1 251,251\n"
"Gvva 156,204,220\n"
"Tv 222,233,11776\n"
"A1 224,224\n"
"Gvva 156,200,204\n"
"WL 2\n"
"Tv 216,233,10240\n"
"A1 225,225\n"
"Gzvva 172,176,12\n"
"Tv 218,233,10752\n"
"A1 226,226\n"
"Gvva 172,180,28\n"
"Tv 212,233,9216\n"
"MU 93,93,125\n"
"Gvva 172,184,44\n"
"Tv 214,233,9728\n"
"MU 94,94,126\n"
"Gvva 172,188,60\n"
"Tv 208,233,8192\n"
"MU 95,95,127\n"
"Gvva 172,192,76\n"
"Tv 210,233,8704\n"
"CV 124,92,93\n"
"Gvva 172,196,92\n"
"s_add_u32 s59, s59, 4096\n"
"buffer_load_dwordx4 v[52:55], v232, s[20:23], s59 offen sc1\n"
"CV 125,94,95\n"
"Gzvva 168,196,88\n"
"Tv 204,233,7168\n"
"MU 88,88,120\n"
"Gvva 168,192,72\n"
"Tv 206,233,7680\n"
"MU 89,89,121\n"
"Gvva 168,188,56\n"
"Tv 200,233,6144\n"
"MU 90,90,122\n"
"Gvva 168,184,40\n"
"Tv 202,233,6656\n"
"MU 91,91,123\n"
"Gvva 168,180,24\n"
"RC 254,254\n"
"Gvva 168,176,8\n"
"RC 251,251\n"
"Gzvva 164,176,4\n"
"RC 224,224\n"
"Gvva 164,180,20\n"
"RC 225,225\n"
"Gvva 164,184,36\n"
"s_add_u32 s59, s59, 4096\n"
"buffer_load_dwordx4 v[56:59], v232, s[20:23], s59 offen sc1\n"
"MU 118,254,118\n"
"Gvva 164,188,52\n"
"RC 226,226\n"
"Gvva 164,192,68\n"
"EN 227,227\n"
"Gvva 164,196,84\n"
"MU 119,251,119\n"
"MU 112,224,112\n"
"Gzvva 160,196,80\n"
"MU 113,225,113\n"
"MU 114,226,114\n"
"Gvva 160,192,64\n"
"A1 227,227\n"
"CV 120,88,89\n"
"Gvva 160,188,48\n"
"Tv 196,233,17408\n"
"CV 121,90,91\n"
"Gvva 160,184,32\n"
"Tv 198,233,17920\n"
"MU 84,84,116\n"
"Gvva 160,180,16\n"
"Tv 192,233,16384\n"
"MU 85,85,117\n"
"Gvva 160,176,0\n"
"WL 3\n"
"Tv 194,233,16896\n"
"MU 86,86,118\n"
"Gvva 160,200,96\n"
"s_add_u32 s59, s59, 4096\n"
"buffer_load_dwordx4 v[60:63], v232, s[20:23], s59 offen sc1\n"
"MU 87,87,119\n"
"Gvva 160,204,112\n"
"Tv 188,233,15360\n"
"CV 116,84,85\n"
"Gvva 160,208,128\n"
"Tv 190,233,15872\n"
"CV 117,86,87\n"
"Gvva 160,212,144\n"
"Tv 184,233,14336\n"
"MU 80,80,112\n"
"Gvva 160,216,160\n"
"Tv 186,233,14848\n"
"MU 81,81,113\n"
"Gvva 160,220,176\n"
"Tv 180,233,13312\n"
"MU 82,82,114\n"
"Gvva 164,220,180\n"
"Tv 182,233,13824\n"
"CV 112,80,81\n"
"Gvva 164,216,164\n"
"Tv 176,233,12288\n"
"FS 254,128,92\n"
"Gvva 164,212,148\n"
"Tv 178,233,12800\n"
"MU 254,254,128\n"
"Gvva 164,208,132\n"
"s_add_u32 s59, s59, 4096\n"
"buffer_load_dwordx4 v[32:35], v232, s[20:23], s59 offen sc1\n"
"FS 251,129,92\n"
"Gvva 164,204,116\n"
"RC 227,227\n"
"Gvva 164,200,100\n"
"EN 254,254\n"
"Gvva 168,200,104\n"
"MU 115,227,115\n"
"MU 83,83,115\n"
"Gvva 168,204,120\n"
"CV 113,82,83\n"
"A1 254,254\n"
"Gvva 168,208,136\n"
"RC 254,254\n"
"Gvva 168,212,152\n"
"MU 128,254,128\n"
"MU 251,251,129\n"
"Gvva 168,216,168\n"
"EN 251,251\n"
"Gvva 168,220,184\n"
"A1 251,251\n"
"FS 224,130,92\n"
"Gvva 172,220,188\n"
"WV 6\n"
"ds_write_b128 v247, v[40:43] offset:8192\n"
"MU 224,224,130\n"
"Gvva 172,216,172\n"
"s_add_u32 s59, s59, 4096\n"
"buffer_load_dwordx4 v[36:39], v232, s[20:23], s59 offen sc1\n"
"FS 225,131,92\n"
"Gvva 172,212,156\n"
"Tv 220,233,23552\n"
"MU 225,225,131\n"
"Gvva 172,208,140\n"
"Tv 222,233,24064\n"
"FS 226,132,92\n"
"Gvva 172,204,124\n"
"Tv 216,233,22528\n"
"MU 226,226,132\n"
"Gvva 172,200,108\n"
"WL 4\n"
"Tv 218,233,23040\n"
"FS 227,133,92\n"
"Gvva 172,176,204\n"
"Tv 212,233,21504\n"
"MU 227,227,133\n"
"Gvva 172,180,220\n"
"Tv 214,233,22016\n"
"FS 254,134,92\n"
"Gvva 172,184,236\n"
"Tv 208,233,20480\n"
"MU 254,254,134\n"
"Gvva 172,188,252\n"
"WV 6\n"
"ds_write_b128 v247, v[44:47] offset:12288\n"
"Gvvv 172,192,12\n"
"s_add_u32 s59, s59, 4096\n"
"buffer_load_dwordx4 v[40:43], v232, s[20:23], s59 offen sc1\n"
"Gvvv 172,196,28\n"
"RC 251,251\n"
"Gvvv 168,196,24\n"
"Tv 210,233,20992\n"
"MU 129,251,129\n"
"Gvvv 168,192,8\n"
"Tv 204,233,19456\n"
"FS 251,135,92\n"
"Gvva 168,188,248\n"
"Tv 206,233,19968\n"
"MU 172,172,248\n"
"Gvva 168,184,232\n"
"Tv 200,233,18432\n"
"MU 173,173,248\n"
"Gvva 168,180,216\n"
"Tv 202,233,18944\n"
"MU 251,251,135\n"
"Gvva 168,176,200\n"
"EN 224,224\n"
"Gvva 164,176,196\n"
"WV 6\n"
"ds_write_b128 v247, v[48:51] offset:16384\n"
"A1 224,224\n"
"Gvva 164,180,212\n"
"RC 224,224\n"
"Gvva 164,184,228\n"
"MU 168,168,248\n"
"MU 169,169,248\n"
"Gvva 164,188,244\n"
"CV 168,168,169\n"
"CV 169,170,171\n"
"Gvvv 164,192,4\n"
"CV 170,172,173\n"
"CV 171,174,175\n"
"Gvvv 164,196,20\n"
"v_permlane16_swap_b32 v168, v170\n"
"v_permlane16_swap_b32 v169, v171\n"
"Gvvv 160,196,16\n"
"ds_write_b32 v234, v170 offset:20880\n"
"MU 130,224,130\n"
"Gvvv 160,192,0\n"
"ds_write_b16 v249, v169 offset:20880\n"
"FS 224,136,92\n"
"Gvva 160,188,240\n"
"ds_write_b16_d16_hi v249, v169 offset:20896\n"
"MU 164,164,248\n"
"Gvva 160,184,224\n"
"WV 5\n"
"ds_write_b128 v247, v[52:55] offset:20480\n"
"MU 165,165,248\n"
"Gvva 160,180,208\n"
"Tv 196,233,29696\n"
"MU 224,224,136\n"
"Gvva 160,176,192\n"
"WL 6\n"
"EN 225,225\n"
"Gzvva 96,200,0\n"
"Tv 198,233,30208\n"
"A1 225,225\n"
"Gvva 96,204,16\n"
"buffer_load_dwordx2 v[228:229], v253, s[20:23], s80 offen sc1\n"
"Gvva 96,208,32\n"
"Tv 192,233,28672\n"
"MU 160,160,248\n"
"Gvva 96,212,48\n"
"Tv 194,233,29184\n"
"MU 161,161,248\n"
"Gvva 96,216,64\n"
"Tv 188,233,27648\n"
"CV 160,160,161\n"
"Gvva 96,220,80\n"
"Tv 190,233,28160\n"
"CV 161,162,163\n"
"Gzvva 100,220,84\n"
"WV 5\n"
"ds_write_b128 v247, v[56:59] offset:24576\n"
"CV 162,164,165\n"
"Gvva 100,216,68\n"
"Tv 184,233,26624\n"
"CV 163,166,167\n"
"Gvva 100,212,52\n"
"Tv 186,233,27136\n"
"v_permlane16_swap_b32 v160, v162\n"
"Gvva 100,208,36\n"
"ds_write_b32 v234, v162 offset:18560\n"
"v_permlane16_swap_b32 v161, v163\n"
"Gvva 100,204,20\n"
"ds_write_b16 v249, v161 offset:18560\n"
"v_add_u32 v234, 148480, v232\n"
"Gvva 100,200,4\n"
"ds_write_b16_d16_hi v249, v161 offset:18576\n"
"Gzvva 104,200,8\n"
"ds_read_b128 v[164:167], v245 offset:18560\n"
"Gvva 104,204,24\n"
"ds_read_b128 v[240:243], v234 offset:4096\n"
"Gvva 104,208,40\n"
"Tv 180,233,25600\n"
"Gvva 104,212,56\n"
"Tv 182,233,26112\n"
"Gvva 104,216,72\n"
"WV 4\n"
"ds_write_b128 v247, v[60:63] offset:28672\n"
"Gvva 104,220,88\n"
"Tv 176,233,24576\n"
"Gzvva 108,220,92\n"
"Tv 178,233,25088\n"
"Gvva 108,216,76\n"
"ds_read_b64 v[238:239], v234 offset:5120\n"
"Gvva 108,212,60\n"
"ds_read_b32 v248, v234 offset:5128\n"
"Gvva 108,208,44\n"
"RC 225,225\n"
"Gvva 108,204,28\n"
"Tv 220,233,35840\n"
"MU 131,225,131\n"
"Gvva 108,200,12\n"
"WL 3\n"
"Tv 222,233,36352\n"
"FS 225,137,92\n"
"Gvva 108,176,108\n"
"Tv 216,233,34816\n"
"MU 225,225,137\n"
"Gvva 108,180,124\n"
"WV 3\n"
"ds_write_b128 v247, v[32:35] offset:32768\n"
"Gvva 108,184,140\n"
"EN 226,226\n"
"Gvva 108,188,156\n"
"Tv 218,233,35328\n"
"A1 226,226\n"
"Gvva 108,192,172\n"
"WL 5\n"
"buffer_store_dwordx4 v[164:167], v250, s[20:23], s65 offen sc1\n"
"v_add_u32 v236, 55296, v239\n"
"Gvva 108,196,188\n"
"RC 226,226\n"
"Gvva 104,196,184\n"
"Tv 212,233,33792\n"
"MU 132,226,132\n"
"Gvva 104,192,168\n"
"Tv 214,233,34304\n"
"FS 226,138,92\n"
"Gvva 104,188,152\n"
"Tv 208,233,32768\n"
"MU 226,226,138\n"
"Gvva 104,184,136\n"
"EN 227,227\n"
"Gvva 104,180,120\n"
"Tv 210,233,33280\n"
"A1 227,227\n"
"Gvva 104,176,104\n"
"WV 3\n"
"ds_write_b128 v247, v[36:39] offset:36864\n"
"Gvva 100,176,100\n"
"RC 227,227\n"
"Gvva 100,180,116\n"
"Tv 204,233,31744\n"
"MU 133,227,133\n"
"Gvva 100,184,132\n"
"Tv 206,233,32256\n"
"FS 227,139,92\n"
"Gvva 100,188,148\n"
"Tv 200,233,30720\n"
"MU 227,227,139\n"
"Gvva 100,192,164\n"
"s_add_u32 s62, s62, 4096\n"
"EN 254,254\n"
"Gvva 100,196,180\n"
"Tv 202,233,31232\n"
"A1 254,254\n"
"Gvva 96,196,176\n"
"RC 254,254\n"
"Gvva 96,192,160\n"
"EN 251,251\n"
"Gvva 96,188,144\n"
"WV 2\n"
"ds_write_b128 v247, v[40:43] offset:40960\n"
"MU 134,254,134\n"
"Gvva 96,184,128\n"
"EN 224,224\n"
"Gvva 96,180,112\n"
"EN 225,225\n"
"Gvva 96,176,96\n"
"WL 1\n"
"EN 226,226\n"
"Gvva 96,200,192\n"
"s_barrier\n"
"Tv 196,233,41984\n"
"Gvva 96,204,208\n"
"s_add_u32 s53, s52, 0\n"
"Tv 198,233,42496\n"
"A1 251,251\n"
"Gvva 96,208,224\n"
"s_and_b32 s53, s53, 3\n"
"Tv 192,233,40960\n"
"A1 224,224\n"
"Gvva 96,212,240\n"
"s_mul_i32 s53, s53, 147456\n"
"Tv 194,233,41472\n"
"A1 225,225\n"
"Gvvv 96,216,0\n"
"s_add_u32 s53, s53, s67\n"
"Tv 188,233,39936\n"
"A1 226,226\n"
"Gvvv 96,220,16\n"
"s_add_u32 m0, s84, 18432\n"
"Tv 190,233,40448\n"
"FS 254,140,92\n"
"Gvvv 100,220,20\n"
"L 240,36,53\n"
"MU 254,254,140\n"
"Gvvv 100,216,4\n"
"Tv 184,233,38912\n"
"MU 144,144,128\n"
"Gvva 100,212,244\n"
"Tv 186,233,39424\n"
"MS 96,98,96\n"
"Gvva 100,208,228\n"
"WV 2\n"
"v_cmp_ne_u64 vcc, s[82:83], v[228:229]\n"
"s_cbranch_vccnz SLH%=\n"
"BKH%=:\n"
"Gvva 100,204,212\n"
"s_add_u32 s54, s52, 0\n"
"Tv 180,233,37888\n"
"MS 97,98,97\n"
"Gvva 100,200,196\n"
"s_and_b32 s54, s54, 3\n"
"Tv 182,233,38400\n"
"MS 98,98,98\n"
"Gvva 104,200,200\n"
"s_lshl_b32 s54, s54, 13\n"
"Tv 176,233,36864\n"
"MS 99,98,99\n"
"Gvva 104,204,216\n"
"s_add_u32 s54, s54, s64\n"
"Tv 178,233,37376\n"
"CV 96,96,97\n"
"Gvva 104,208,232\n"
"MS 100,98,100\n"
"MS 101,98,101\n"
"Gvva 104,212,248\n"
"s_add_u32 m0, s84, 19456\n"
"MS 102,98,102\n"
"MS 103,98,103\n"
"Gvvv 104,216,8\n"
"L 241,36,53\n"
"CV 97,98,99\n"
"Gvvv 104,220,24\n"
"CV 98,100,101\n"
"CV 99,102,103\n"
"Gvvv 108,220,28\n"
"v_permlane16_swap_b32 v96, v98\n"
"v_permlane16_swap_b32 v97, v99\n"
"Gvvv 108,216,12\n"
"buffer_store_dwordx4 v[96:99], v232, s[20:23], s65 offen sc1\n"
"MU 145,145,129\n"
"Gvva 108,212,252\n"
"ds_write_b128 v244, v[96:99] offset:18560\n"
"MS 104,98,104\n"
"Gvva 108,208,236\n"
"Tv 220,233,48128\n"
"MS 105,98,105\n"
"Gvva 108,204,220\n"
"Tv 222,233,48640\n"
"MS 106,98,106\n"
"Gvva 108,200,204\n"
"WL 3\n"
"Tv 216,233,47104\n"
"MS 107,98,107\n"
"Gzvav 76,12,176\n"
"Tv 218,233,47616\n"
"CV 104,104,105\n"
"Gvav 76,28,180\n"
"Tv 212,233,46080\n"
"CV 105,106,107\n"
"Gvav 76,44,184\n"
"Tv 214,233,46592\n"
"MS 108,98,108\n"
"Gvav 76,60,188\n"
"Tv 208,233,45056\n"
"MS 109,98,109\n"
"Gvav 76,76,192\n"
"Tv 210,233,45568\n"
"MS 110,98,110\n"
"Gvav 76,92,196\n"
"s_add_u32 m0, s84, 20480\n"
"Tv 204,233,44032\n"
"MS 111,98,111\n"
"Gzvav 72,88,196\n"
"L 242,36,53\n"
"CV 106,108,109\n"
"Gvav 72,72,192\n"
"Tv 206,233,44544\n"
"CV 107,110,111\n"
"Gvav 72,56,188\n"
"Tv 200,233,43008\n"
"v_permlane16_swap_b32 v104, v106\n"
"Gvav 72,40,184\n"
"Tv 202,233,43520\n"
"v_permlane16_swap_b32 v105, v107\n"
"Gvav 72,24,180\n"
"buffer_store_dwordx4 v[104:107], v232, s[20:23], s65 offen offset:2304 sc1\n"
"MU 146,146,130\n"
"Gvav 72,8,176\n"
"ds_write_b128 v244, v[104:107] offset:20880\n"
"MU 147,147,131\n"
"Gzvav 68,4,176\n"
"RC 251,251\n"
"Gvav 68,20,180\n"
"RC 224,224\n"
"Gvav 68,36,184\n"
"RC 225,225\n"
"Gvav 68,52,188\n"
"RC 226,226\n"
"Gvav 68,68,192\n"
"EN 227,227\n"
"Gvav 68,84,196\n"
"EN 254,254\n"
"Gzvav 64,80,196\n"
"MU 135,251,135\n"
"MU 136,224,136\n"
"Gvav 64,64,192\n"
"s_add_u32 m0, s84, 21504\n"
"MU 137,225,137\n"
"MU 138,226,138\n"
"Gvav 64,48,188\n"
"L 243,36,53\n"
"A1 227,227\n"
"Gvav 64,32,184\n"
"Tv 196,233,54272\n"
"A1 254,254\n"
"Gvav 64,16,180\n"
"Tv 198,233,54784\n"
"FS 251,141,92\n"
"Gvav 64,0,176\n"
"WL 3\n"
"Tv 192,233,53248\n"
"MU 251,251,141\n"
"Gvav 64,96,200\n"
"Tv 194,233,53760\n"
"FS 224,142,92\n"
"Gvav 64,112,204\n"
"Tv 188,233,52224\n"
"MU 224,224,142\n"
"Gvav 64,128,208\n"
"Tv 190,233,52736\n"
"FS 225,143,92\n"
"Gvav 64,144,212\n"
"Tv 184,233,51200\n"
"MU 225,225,143\n"
"Gvav 64,160,216\n"
"Tv 186,233,51712\n"
"CV 114,144,145\n"
"Gvav 64,176,220\n"
"Tv 180,233,50176\n"
"CV 115,146,147\n"
"Gvav 68,180,220\n"
"buffer_store_dwordx4 v[112:115], v232, s[20:23], s62 offen sc1\n"
"MU 148,148,132\n"
"Gvav 68,164,216\n"
"Tv 182,233,50688\n"
"MU 149,149,133\n"
"Gvav 68,148,212\n"
"Tv 176,233,49152\n"
"MU 150,150,134\n"
"Gvav 68,132,208\n"
"s_add_u32 m0, s84, 22528\n"
"Tv 178,233,49664\n"
"MU 151,151,135\n"
"Gvav 68,116,204\n"
"s_mov_b32 exec_hi, 0\n"
"L 238,36,53\n"
"s_mov_b32 exec_hi, -1\n"
"Gvav 68,100,200\n"
"RC 227,227\n"
"Gvav 72,104,200\n"
"RC 254,254\n"
"Gvav 72,120,204\n"
"EN 251,251\n"
"Gvav 72,136,208\n"
"EN 224,224\n"
"Gvav 72,152,212\n"
"EN 225,225\n"
"Gvav 72,168,216\n"
"MU 139,227,139\n"
"MU 140,254,140\n"
"Gvav 72,184,220\n"
"A1 251,251\n"
"A1 224,224\n"
"Gvav 76,188,220\n"
"RC 251,251\n"
"Gvav 76,172,216\n"
"RC 224,224\n"
"Gvav 76,156,212\n"
"Tv 220,237,5120\n"
"MU 141,251,141\n"
"Gvav 76,140,208\n"
"Tv 222,237,5632\n"
"MU 142,224,142\n"
"Gvav 76,124,204\n"
"Tv 216,237,4096\n"
"A1 225,225\n"
"Gvav 76,108,200\n"
"WL 3\n"
"Tv 218,237,4608\n"
"CV 118,148,149\n"
"Gvav 76,204,176\n"
"Tv 212,237,3072\n"
"CV 119,150,151\n"
"Gvav 76,220,180\n"
"s_add_u32 s53, s52, 1\n"
"buffer_store_dwordx4 v[116:119], v232, s[20:23], s62 offen offset:1024 sc1\n"
"MU 152,152,136\n"
"Gvav 76,236,184\n"
"s_and_b32 s53, s53, 3\n"
"Tv 214,237,3584\n"
"MU 153,153,137\n"
"Gvav 76,252,188\n"
"s_mul_i32 s53, s53, 147456\n"
"Tv 208,237,2048\n"
"MU 154,154,138\n"
"Gvvv 76,12,192\n"
"s_add_u32 s53, s53, s67\n"
"Tv 210,237,2560\n"
"MU 155,155,139\n"
"Gvvv 76,28,196\n"
"s_add_u32 m0, s84, 0\n"
"Tv 204,237,1024\n"
"CV 122,152,153\n"
"Gvvv 72,24,196\n"
"L 240,36,53\n"
"CV 123,154,155\n"
"Gvvv 72,8,192\n"
"buffer_store_dwordx4 v[120:123], v232, s[20:23], s62 offen offset:2048 sc1\n"
"MU 156,156,140\n"
"Gvav 72,248,188\n"
"Tv 206,237,1536\n"
"MU 157,157,141\n"
"Gvav 72,232,184\n"
"Tv 200,237,0\n"
"MU 158,158,142\n"
"Gvav 72,216,180\n"
"Tv 202,237,512\n"
"CV 126,156,157\n"
"Gvav 72,200,176\n"
"RC 225,225\n"
"Gvav 68,196,176\n"
"MU 143,225,143\n"
"MU 159,159,143\n"
"Gvav 68,212,180\n"
"CV 127,158,159\n"
"v_lshlrev_b32 v247, 4, v233\n"
"Gvav 68,228,184\n"
"buffer_store_dwordx4 v[124:127], v232, s[20:23], s62 offen offset:3072 sc1\n"
"CV 72,72,73\n"
"Gvav 68,244,188\n"
"s_add_u32 m0, s84, 1024\n"
"CV 73,74,75\n"
"CV 74,76,77\n"
"Gvvv 68,4,192\n"
"L 241,36,53\n"
"CV 75,78,79\n"
"Gvvv 68,20,196\n"
"v_permlane16_swap_b32 v72, v74\n"
"v_permlane16_swap_b32 v73, v75\n"
"Gvvv 64,16,196\n"
"buffer_store_dwordx4 v[72:75], v232, s[20:23], s65 offen offset:3328 sc1\n"
"Gvvv 64,0,192\n"
"ds_write_b128 v244, v[72:75] offset:21904\n"
"Gvav 64,240,188\n"
"Tv 196,237,11264\n"
"Gvav 64,224,184\n"
"Tv 198,237,11776\n"
"Gvav 64,208,180\n"
"Tv 192,237,10240\n"
"Gvav 64,192,176\n"
"WL 4\n"
"Tv 194,237,10752\n"
"Gzvva 80,200,0\n"
"Tv 188,237,9216\n"
"Gvva 80,204,16\n"
"Tv 190,237,9728\n"
"Gvva 80,208,32\n"
"Tv 184,237,8192\n"
"CV 64,64,65\n"
"Gvva 80,212,48\n"
"Tv 186,237,8704\n"
"CV 65,66,67\n"
"Gvva 80,216,64\n"
"Tv 180,237,7168\n"
"CV 66,68,69\n"
"Gvva 80,220,80\n"
"s_add_u32 m0, s84, 2048\n"
"Tv 182,237,7680\n"
"CV 67,70,71\n"
"Gzvva 84,220,84\n"
"L 242,36,53\n"
"v_permlane16_swap_b32 v64, v66\n"
"Gvva 84,216,68\n"
"Tv 176,237,6144\n"
"v_permlane16_swap_b32 v65, v67\n"
"Gvva 84,212,52\n"
"buffer_store_dwordx4 v[64:67], v232, s[20:23], s65 offen offset:1024 sc1\n"
"Gvva 84,208,36\n"
"ds_write_b128 v244, v[64:67] offset:19584\n"
"Gvva 84,204,20\n"
"Tv 178,237,6656\n"
"Gvva 84,200,4\n"
"Gzvva 88,200,8\n"
"Gvva 88,204,24\n"
"Gvva 88,208,40\n"
"Gvva 88,212,56\n"
"Gvva 88,216,72\n"
"Gvva 88,220,88\n"
"Gzvva 92,220,92\n"
"Gvva 92,216,76\n"
"s_add_u32 m0, s84, 3072\n"
"Gvva 92,212,60\n"
"L 243,36,53\n"
"Gvva 92,208,44\n"
"Tv 220,237,17408\n"
"Gvva 92,204,28\n"
"Tv 222,237,17920\n"
"Gvva 92,200,12\n"
"WL 2\n"
"Tv 216,237,16384\n"
"Gvva 92,176,108\n"
"Tv 218,237,16896\n"
"Gvva 92,180,124\n"
"Tv 212,237,15360\n"
"Gvva 92,184,140\n"
"Tv 214,237,15872\n"
"Gvva 92,188,156\n"
"Tv 208,237,14336\n"
"Gvva 92,192,172\n"
"Tv 210,237,14848\n"
"Gvva 92,196,188\n"
"Tv 204,237,13312\n"
"Gvva 88,196,184\n"
"Tv 206,237,13824\n"
"Gvva 88,192,168\n"
"Tv 200,237,12288\n"
"Gvva 88,188,152\n"
"Tv 202,237,12800\n"
"Gvva 88,184,136\n"
"s_add_u32 m0, s84, 4096\n"
"Gvva 88,180,120\n"
"s_mov_b32 exec_hi, 0\n"
"L 238,36,53\n"
"s_mov_b32 exec_hi, -1\n"
"Gvva 88,176,104\n"
"Gvva 84,176,100\n"
"Gvva 84,180,116\n"
"Gvva 84,184,132\n"
"Gvva 84,188,148\n"
"Gvva 84,192,164\n"
"Gvva 84,196,180\n"
"Gvva 80,196,176\n"
"Gvva 80,192,160\n"
"Gvva 80,188,144\n"
"Gvva 80,184,128\n"
"Gvva 80,180,112\n"
"Gvva 80,176,96\n"
"WL 0\n"
"Gvva 80,200,192\n"
"s_barrier\n"
"Tv 196,237,35840\n"
"Gvva 80,204,208\n"
"s_add_u32 s53, s52, 2\n"
"Tv 198,237,36352\n"
"Gvva 80,208,224\n"
"s_and_b32 s53, s53, 3\n"
"Tv 192,237,34816\n"
"Gvva 80,212,240\n"
"s_mul_i32 s53, s53, 147456\n"
"Tv 194,237,35328\n"
"Gvvv 80,216,0\n"
"s_add_u32 s53, s53, s67\n"
"ds_read_b128 v[224:227], v234 offset:6144\n"
"v_mov_b32 v254, 0xdddddddd\n"
"Gvvv 80,220,16\n"
"s_add_u32 m0, s84, 55296\n"
"Tv 188,237,33792\n"
"Gvvv 84,220,20\n"
"L 240,36,53\n"
"Gvvv 84,216,4\n"
"Tv 190,237,34304\n"
"Gvva 84,212,244\n"
"Tv 184,237,32768\n"
"FS 144,80,92\n"
"Gvva 84,208,228\n"
"Tv 186,237,33280\n"
"MU 144,144,80\n"
"Gvva 84,204,212\n"
"Tv 180,237,31744\n"
"FS 145,81,92\n"
"Gvva 84,200,196\n"
"Tv 182,237,32256\n"
"MU 145,145,81\n"
"Gvva 88,200,200\n"
"Tv 176,237,30720\n"
"FS 146,82,92\n"
"Gvva 88,204,216\n"
"Tv 178,237,31232\n"
"MU 146,146,82\n"
"Gvva 88,208,232\n"
"EN 144,144\n"
"Gvva 88,212,248\n"
"s_add_u32 m0, s84, 56320\n"
"EN 145,145\n"
"Gvvv 88,216,8\n"
"L 241,36,53\n"
"A1 144,144\n"
"Gvvv 88,220,24\n"
"RC 144,144\n"
"Gvvv 92,220,28\n"
"EN 146,146\n"
"Gvvv 92,216,12\n"
"MU 80,144,80\n"
"A1 145,145\n"
"Gvva 92,212,252\n"
"Tv 220,237,54272\n"
"A1 146,146\n"
"Gvva 92,208,236\n"
"Tv 222,237,54784\n"
"FS 147,83,92\n"
"Gvva 92,204,220\n"
"Tv 216,237,53248\n"
"MU 147,147,83\n"
"Gvva 92,200,204\n"
"WL 3\n"
"Tv 218,237,53760\n"
"FS 148,84,92\n"
"Gzvva 124,176,204\n"
"Tv 212,237,52224\n"
"MU 148,148,84\n"
"Gvva 124,180,220\n"
"Tv 214,237,52736\n"
"FS 149,85,92\n"
"Gvva 124,184,236\n"
"Tv 208,237,51200\n"
"MU 149,149,85\n"
"Gvva 124,188,252\n"
"Tv 210,237,51712\n"
"FS 150,86,92\n"
"Gvvv 124,192,12\n"
"Tv 204,237,50176\n"
"MU 150,150,86\n"
"Gvvv 124,196,28\n"
"s_add_u32 m0, s84, 57344\n"
"Tv 206,237,50688\n"
"FS 151,87,92\n"
"Gzvvv 120,196,24\n"
"L 242,36,53\n"
"MU 151,151,87\n"
"Gvvv 120,192,8\n"
"Tv 200,237,49152\n"
"FS 144,88,92\n"
"Gvva 120,188,248\n"
"Tv 202,237,49664\n"
"MU 144,144,88\n"
"Gvva 120,184,232\n"
"RC 145,145\n"
"Gvva 120,180,216\n"
"RC 146,146\n"
"Gvva 120,176,200\n"
"EN 147,147\n"
"Gzvva 116,176,196\n"
"WV 5\n"
"buffer_store_dwordx2 v[230:231], v252, s[20:23], s75 offen offset:32 sc1\n"
"MU 81,145,81\n"
"Gvva 116,180,212\n"
"EN 148,148\n"
"Gvva 116,184,228\n"
"EN 149,149\n"
"Gvva 116,188,244\n"
"EN 150,150\n"
"Gvvv 116,192,4\n"
"EN 151,151\n"
"Gvvv 116,196,20\n"
"EN 144,144\n"
"Gzvvv 112,196,16\n"
"MU 82,146,82\n"
"A1 147,147\n"
"Gvvv 112,192,0\n"
"s_add_u32 m0, s84, 58368\n"
"RC 147,147\n"
"Gvva 112,188,240\n"
"L 243,36,53\n"
"MU 83,147,83\n"
"Gvva 112,184,224\n"
"Tv 196,237,23552\n"
"A1 148,148\n"
"Gvva 112,180,208\n"
"Tv 198,237,24064\n"
"A1 149,149\n"
"Gvva 112,176,192\n"
"WL 2\n"
"Tv 192,237,22528\n"
"A1 150,150\n"
"Gzvva 128,200,192\n"
"Tv 194,237,23040\n"
"A1 151,151\n"
"Gvva 128,204,208\n"
"Tv 188,237,21504\n"
"A1 144,144\n"
"Gvva 128,208,224\n"
"Tv 190,237,22016\n"
"FS 145,89,92\n"
"Gvva 128,212,240\n"
"Tv 184,237,20480\n"
"MU 145,145,89\n"
"Gvvv 128,216,0\n"
"Tv 186,237,20992\n"
"FS 146,90,92\n"
"Gvvv 128,220,16\n"
"Tv 180,237,19456\n"
"MU 146,146,90\n"
"Gzvvv 132,220,20\n"
"Tv 182,237,19968\n"
"FS 147,91,92\n"
"Gvvv 132,216,4\n"
"Tv 176,237,18432\n"
"MU 147,147,91\n"
"Gvva 132,212,244\n"
"RC 148,148\n"
"Gvva 132,208,228\n"
"s_add_u32 m0, s84, 59392\n"
"Tv 178,237,18944\n"
"MU 84,148,84\n"
"Gvva 132,204,212\n"
"s_mov_b32 exec_hi, 0\n"
"L 238,36,53\n"
"s_mov_b32 exec_hi, -1\n"
"Gvva 132,200,196\n"
"RC 149,149\n"
"Gzvva 136,200,200\n"
"RC 150,150\n"
"Gvva 136,204,216\n"
"RC 151,151\n"
"Gvva 136,208,232\n"
"RC 144,144\n"
"Gvva 136,212,248\n"
"EN 145,145\n"
"Gvvv 136,216,8\n"
"EN 146,146\n"
"Gvvv 136,220,24\n"
"EN 147,147\n"
"Gzvvv 140,220,28\n"
"MU 85,149,85\n"
"MU 86,150,86\n"
"Gvvv 140,216,12\n"
"MU 87,151,87\n"
"MU 88,144,88\n"
"Gvva 140,212,252\n"
"Tv 220,237,41984\n"
"v_mov_b32 v0, 0\n"
"Gvva 140,208,236\n"
"Tv 222,237,42496\n"
"v_mov_b32 v1, 0\n"
"Gvva 140,204,220\n"
"Tv 216,237,40960\n"
"v_mov_b32 v2, 0\n"
"Gvva 140,200,204\n"
"WL 3\n"
"Tv 218,237,41472\n"
"v_mov_b32 v3, 0\n"
"Gvva 124,176,12\n"
"buffer_load_dwordx4 a[192:195], v232, s[20:23], s54 offen sc1\n"
"v_mov_b32 v4, 0\n"
"Gvva 124,180,28\n"
"Tv 212,237,39936\n"
"v_mov_b32 v5, 0\n"
"Gvva 124,184,44\n"
"Tv 214,237,40448\n"
"v_mov_b32 v6, 0\n"
"Gvva 124,188,60\n"
"Tv 208,237,38912\n"
"v_mov_b32 v7, 0\n"
"Gvva 124,192,76\n"
"Tv 210,237,39424\n"
"v_mov_b32 v8, 0\n"
"Gvva 124,196,92\n"
"buffer_load_dwordx4 a[196:199], v232, s[20:23], s54 offen offset:1024 sc1\n"
"v_mov_b32 v9, 0\n"
"Gvva 120,196,88\n"
"Tv 204,237,37888\n"
"v_mov_b32 v10, 0\n"
"Gvva 120,192,72\n"
"Tv 206,237,38400\n"
"v_mov_b32 v16, 0\n"
"Gvva 120,188,56\n"
"Tv 200,237,36864\n"
"v_mov_b32 v17, 0\n"
"Gvva 120,184,40\n"
"Tv 202,237,37376\n"
"v_mov_b32 v18, 0\n"
"Gvva 120,180,24\n"
"buffer_load_dwordx4 a[200:203], v232, s[20:23], s54 offen offset:2048 sc1\n"
"v_mov_b32 v19, 0\n"
"Gvva 120,176,8\n"
"v_mov_b32 v20, 0\n"
"A1 145,145\n"
"Gvva 116,176,4\n"
"v_mov_b32 v21, 0\n"
"A1 146,146\n"
"Gvva 116,180,20\n"
"v_mov_b32 v22, 0\n"
"A1 147,147\n"
"Gvva 116,184,36\n"
"v_mov_b32 v23, 0\n"
"FS 148,92,92\n"
"Gvva 116,188,52\n"
"buffer_load_dwordx4 a[204:207], v232, s[20:23], s54 offen offset:3072 sc1\n"
"v_mov_b32 v24, 0\n"
"Gvva 116,192,68\n"
"v_mov_b32 v25, 0\n"
"MU 148,148,92\n"
"Gvva 116,196,84\n"
"v_mov_b32 v26, 0\n"
"FS 149,93,92\n"
"Gvva 112,196,80\n"
"RC 145,145\n"
"Gvva 112,192,64\n"
"RC 146,146\n"
"Gvva 112,188,48\n"
"s_add_u32 s54, s54, 4096\n"
"buffer_load_dwordx4 a[208:211], v232, s[20:23], s54 offen sc1\n"
"MU 89,145,89\n"
"Gvva 112,184,32\n"
"Tv 196,237,29696\n"
"MU 90,146,90\n"
"Gvva 112,180,16\n"
"Tv 198,237,30208\n"
"MU 149,149,93\n"
"Gvva 112,176,0\n"
"WL 2\n"
"Tv 192,237,28672\n"
"FS 150,94,92\n"
"Gvva 128,200,0\n"
"Tv 194,237,29184\n"
"MU 150,150,94\n"
"Gvva 128,204,16\n"
"buffer_load_dwordx4 a[212:215], v232, s[20:23], s54 offen offset:1024 sc1\n"
"FS 151,95,92\n"
"Gvva 128,208,32\n"
"Tv 188,237,27648\n"
"MU 151,151,95\n"
"Gvva 128,212,48\n"
"RC 147,147\n"
"Gvva 128,216,64\n"
"Tv 190,237,28160\n"
"MU 91,147,91\n"
"Gvva 128,220,80\n"
"EN 148,148\n"
"Gvva 132,220,84\n"
"buffer_load_dwordx4 a[216:219], v232, s[20:23], s54 offen offset:2048 sc1\n"
"A1 148,148\n"
"Gvva 132,216,68\n"
"RC 148,148\n"
"Gvva 132,212,52\n"
"Tv 184,237,26624\n"
"MU 92,148,92\n"
"Gvva 132,208,36\n"
"EN 149,149\n"
"Gvva 132,204,20\n"
"Tv 186,237,27136\n"
"A1 149,149\n"
"Gvva 132,200,4\n"
"buffer_load_dwordx4 a[220:223], v232, s[20:23], s54 offen offset:3072 sc1\n"
"Gvva 136,200,8\n"
"s_add_u32 s54, s52, 1\n"
"RC 149,149\n"
"Gvva 136,204,24\n"
"s_and_b32 s54, s54, 3\n"
"Tv 180,237,25600\n"
"MU 93,149,93\n"
"Gvva 136,208,40\n"
"s_lshl_b32 s54, s54, 13\n"
"EN 150,150\n"
"Gvva 136,212,56\n"
"s_add_u32 s54, s54, s64\n"
"Tv 182,237,26112\n"
"A1 150,150\n"
"Gvva 136,216,72\n"
"buffer_load_dwordx4 a[224:227], v232, s[20:23], s54 offen sc1\n"
"Gvva 136,220,88\n"
"Tv 176,237,24576\n"
"Gvva 140,220,92\n"
"Tv 178,237,25088\n"
"Gvva 140,216,76\n"
"RC 150,150\n"
"Gvva 140,212,60\n"
"Tv 220,237,48128\n"
"MU 94,150,94\n"
"Gvva 140,208,44\n"
"EN 151,151\n"
"Gvva 140,204,28\n"
"Tv 222,237,48640\n"
"A1 151,151\n"
"Gvva 140,200,12\n"
"WL 2\n"
"Tv 216,237,47104\n"
"v_accvgpr_write_b32 a28, 0\n"
"Gvva 124,176,108\n"
"ds_read_b32 a24, v227 offset:18560\n"
"v_accvgpr_write_b32 a29, 0\n"
"Gvva 124,180,124\n"
"ds_read_b128 a[0:3], v224 offset:18560\n"
"v_accvgpr_write_b32 a30, 0\n"
"Gvva 124,184,140\n"
"ds_read_b128 a[4:7], v224 offset:20880\n"
"v_accvgpr_write_b32 a31, 0\n"
"Gvva 124,188,156\n"
"buffer_load_dwordx4 a[228:231], v232, s[20:23], s54 offen offset:1024 sc1\n"
"v_accvgpr_write_b32 a32, 0\n"
"Gvva 124,192,172\n"
"Tv 218,237,47616\n"
"v_accvgpr_write_b32 a33, 0\n"
"Gvva 124,196,188\n"
"Tv 212,237,46080\n"
"v_accvgpr_write_b32 a34, 0\n"
"Gvva 120,196,184\n"
"Tv 214,237,46592\n"
"v_accvgpr_write_b32 a35, 0\n"
"Gvva 120,192,168\n"
"Tv 208,237,45056\n"
"v_accvgpr_write_b32 a25, 0\n"
"Gvva 120,188,152\n"
"Tv 210,237,45568\n"
"v_accvgpr_write_b32 a27, 0\n"
"Gvva 120,184,136\n"
"Tv 204,237,44032\n"
"MU 124,124,92\n"
"Gvva 120,180,120\n"
"Tv 206,237,44544\n"
"MU 125,125,93\n"
"Gvva 120,176,104\n"
"Tv 200,237,43008\n"
"MU 126,126,94\n"
"Gvva 116,176,100\n"
"buffer_load_dwordx4 a[232:235], v232, s[20:23], s54 offen offset:2048 sc1\n"
"v_accvgpr_write_b32 a36, s99\n"
"Gvva 116,180,116\n"
"Tv 202,237,43520\n"
"v_accvgpr_write_b32 a37, s99\n"
"Gvva 116,184,132\n"
"v_accvgpr_write_b32 a38, s99\n"
"MU 120,120,88\n"
"Gvva 116,188,148\n"
"v_accvgpr_write_b32 a39, s99\n"
"MU 121,121,89\n"
"Gvva 116,192,164\n"
"RC 151,151\n"
"Gvva 116,196,180\n"
"MU 95,151,95\n"
"MU 122,122,90\n"
"Gvva 112,196,176\n"
"MU 123,123,91\n"
"MU 127,127,95\n"
"Gvva 112,192,160\n"
"CV 120,120,121\n"
"CV 121,122,123\n"
"Gvva 112,188,144\n"
"s_mov_b32 exec_hi, 0\n"
"v_accvgpr_write_b32 a28, v160\n"
"v_accvgpr_write_b32 a32, v168\n"
"s_mov_b32 exec_hi, -1\n"
"Gvva 112,184,128\n"
"s_mov_b32 exec_lo, 0\n"
"v_accvgpr_write_b32 a30, v160\n"
"v_accvgpr_write_b32 a34, v168\n"
"s_mov_b32 exec_lo, -1\n"
"Gvva 112,180,112\n"
"buffer_load_dwordx4 a[236:239], v232, s[20:23], s54 offen offset:3072 sc1\n"
"CV 122,124,125\n"
"Gvva 112,176,96\n"
"s_waitcnt vmcnt(18) lgkmcnt(0)\n"
"CV 123,126,127\n"
"MU 116,116,84\n"
"Gvva 128,200,96\n"
"s_barrier\n"
"buffer_store_dwordx4 v[120:123], v232, s[20:23], s63 offen offset:1024 sc1\n"
"Gvva 128,204,112\n"
"Ta 64,239,18432\n"
"MU 117,117,85\n"
"Gvva 128,208,128\n"
"Ta 66,239,23040\n"
"MU 112,112,80\n"
"Gvva 128,212,144\n"
"Ta 68,239,27648\n"
"MU 113,113,81\n"
"Gvva 128,216,160\n"
"Ta 70,239,32256\n"
"MU 114,114,82\n"
"Gvva 128,220,176\n"
"Ta 72,239,18464\n"
"MU 115,115,83\n"
"Gvva 132,220,180\n"
"Ta 74,239,23072\n"
"MU 118,118,86\n"
"Gvva 132,216,164\n"
"Ta 76,239,27680\n"
"MU 119,119,87\n"
"Gvva 132,212,148\n"
"s_add_u32 s54, s54, 4096\n"
"Ta 78,239,32288\n"
"CV 128,128,129\n"
"Gvva 132,208,132\n"
"Ta 80,239,18496\n"
"CV 129,130,131\n"
"Gvva 132,204,116\n"
"Ta 82,239,23104\n"
"CV 112,112,113\n"
"Gvva 132,200,100\n"
"Ta 84,239,27712\n"
"CV 113,114,115\n"
"Gvva 136,200,104\n"
"Ta 86,239,32320\n"
"CV 114,116,117\n"
"Gvva 136,204,120\n"
"ds_write_b128 v248, a[36:39] offset:18560\n"
"CV 115,118,119\n"
"Gvva 136,208,136\n"
"ds_write_b128 v248, a[36:39] offset:37120\n"
"CV 130,132,133\n"
"Gvva 136,212,152\n"
"ds_write_b128 v248, a[36:39] offset:0\n"
"CV 131,134,135\n"
"Gvva 136,216,168\n"
"buffer_load_dwordx4 a[240:243], v232, s[20:23], s54 offen sc1\n"
"v_permlane16_swap_b32 v128, v130\n"
"Gvva 136,220,184\n"
"buffer_store_dwordx4 v[112:115], v232, s[20:23], s63 offen sc1\n"
"v_permlane16_swap_b32 v129, v131\n"
"Gvva 140,220,188\n"
"buffer_load_dwordx4 a[244:247], v232, s[20:23], s54 offen offset:1024 sc1\n"
"v_accvgpr_write_b32 a56, v128\n"
"Gvva 140,216,172\n"
"v_accvgpr_write_b32 a58, v130\n"
"v_accvgpr_write_b32 a57, v129\n"
"Gvva 140,212,156\n"
"v_accvgpr_write_b32 a59, v131\n"
"CV 136,136,137\n"
"Gvva 140,208,140\n"
"CV 137,138,139\n"
"Gvva 140,204,124\n"
"buffer_load_dwordx2 v[228:229], v235, s[20:23], s79 offen offset:32 sc1\n"
"Gvva 140,200,108\n"
"Nzvaa 176,24,28\n"
"s_nop 9\n"
"Mvaa 176,0,56\n"
"s_waitcnt vmcnt(12) lgkmcnt(11)\n"
"Gzvaa 32,64,192\n"
"ds_read_b128 a[8:11], v225 offset:18560\n"
"CV 138,140,141\n"
"Gvaa 32,68,208\n"
"WV 11\n"
"ds_read_b128 a[12:15], v226 offset:18560\n"
"CV 139,142,143\n"
"Gzvaa 36,68,212\n"
"buffer_load_dwordx4 a[248:251], v232, s[20:23], s54 offen offset:2048 sc1\n"
"v_permlane16_swap_b32 v136, v138\n"
"Gvaa 36,64,196\n"
"WV 11\n"
"v_permlane16_swap_b32 v137, v139\n"
"v_accvgpr_write_b32 a60, v136\n"
"Gzvaa 40,64,200\n"
"v_accvgpr_write_b32 a62, v138\n"
"v_accvgpr_write_b32 a61, v137\n"
"Gvaa 40,68,216\n"
"WV 10\n"
"v_accvgpr_write_b32 a63, v139\n"
"Gzvaa 44,68,220\n"
"Gvaa 44,64,204\n"
"WL 9\n"
"Nzvaa 192,24,32\n"
"ds_read_b32 a26, v227 offset:23200\n"
"Ta 88,239,18528\n"
"Ta 90,239,23136\n"
"E 176,176\n"
"E 177,177\n"
"E 178,178\n"
"E 179,179\n"
"Gzvaa 60,72,204\n"
"E 180,180\n"
"Gvaa 60,76,220\n"
"E 181,181\n"
"Gzvaa 56,76,216\n"
"E 182,182\n"
"Gvaa 56,72,200\n"
"buffer_load_dwordx4 a[252:255], v232, s[20:23], s54 offen offset:3072 sc1\n"
"E 183,183\n"
"Mvaa 192,0,60\n"
"s_add_u32 s54, s52, 2\n"
"Ta 92,239,27744\n"
"Ta 94,239,32352\n"
"E 184,184\n"
"E 185,185\n"
"E 186,186\n"
"E 187,187\n"
"Gzvaa 52,72,196\n"
"s_and_b32 s54, s54, 3\n"
"E 188,188\n"
"Gvaa 52,76,212\n"
"s_lshl_b32 s54, s54, 13\n"
"E 189,189\n"
"Gzvaa 48,76,208\n"
"s_add_u32 s54, s54, s64\n"
"buffer_load_dwordx4 a[96:99], v232, s[20:23], s54 offen sc1\n"
"E 190,190\n"
"Gvaa 48,72,192\n"
"WL 5\n"
"buffer_load_dwordx4 a[100:103], v232, s[20:23], s54 offen offset:1024 sc1\n"
"E 191,191\n"
"Nzvaa 208,24,30\n"
"ds_read_b128 a[16:19], v225 offset:20880\n"
"ds_read_b128 a[20:23], v226 offset:20880\n"
"E 192,192\n"
"E 193,193\n"
"Gzvaa 64,80,192\n"
"E 194,194\n"
"Gvaa 64,84,208\n"
"E 195,195\n"
"Svav 0,8,176,254\n"
"ds_read_b128 a[0:3], v224 offset:23200\n"
"Ta 64,239,18560\n"
"E 196,196\n"
"E 197,197\n"
"Gzvaa 68,84,212\n"
"E 198,198\n"
"Gvaa 68,80,196\n"
"E 199,199\n"
"Mvaa 208,4,56\n"
"Ta 66,239,23168\n"
"Ta 68,239,27776\n"
"E 200,200\n"
"E 201,201\n"
"Gzvaa 72,80,200\n"
"buffer_load_dwordx4 a[104:107], v232, s[20:23], s54 offen offset:2048 sc1\n"
"E 202,202\n"
"Gvaa 72,84,216\n"
"E 203,203\n"
"Svav 0,12,184,254\n"
"Ta 70,239,32384\n"
"E 204,204\n"
"E 205,205\n"
"Gzvaa 76,84,220\n"
"E 206,206\n"
"Gvaa 76,80,204\n"
"WL 7\n"
"E 207,207\n"
"Nzvaa 176,24,34\n"
"Ta 72,239,18592\n"
"Ta 74,239,23200\n"
"E 208,208\n"
"E 209,209\n"
"Gzvaa 108,88,204\n"
"E 210,210\n"
"Gvaa 108,92,220\n"
"E 211,211\n"
"Svav 16,8,192,254\n"
"Ta 76,239,27808\n"
"Ta 78,239,32416\n"
"E 212,212\n"
"E 213,213\n"
"Gzvaa 104,92,216\n"
"buffer_load_dwordx4 a[108:111], v232, s[20:23], s54 offen offset:3072 sc1\n"
"E 214,214\n"
"Gvaa 104,88,200\n"
"E 215,215\n"
"Mvaa 176,4,60\n"
"E 216,216\n"
"E 217,217\n"
"Gzvaa 100,88,196\n"
"E 218,218\n"
"Gvaa 100,92,212\n"
"E 219,219\n"
"Svav 16,12,200,254\n"
"WV 8\n"
"buffer_store_dwordx2 v[230:231], v252, s[20:23], s76 offen offset:32 sc1\n"
"E 220,220\n"
"E 221,221\n"
"Gzvaa 96,92,208\n"
"E 222,222\n"
"Gvaa 96,88,192\n"
"WL 4\n"
"E 223,223\n"
"Nzvaa 192,26,28\n"
"WV 7\n"
"ds_read_b128 a[8:11], v225 offset:23200\n"
"ds_read_b128 a[12:15], v226 offset:23200\n"
"E 176,176\n"
"E 177,177\n"
"Gzvaa 144,64,192\n"
"v_cmp_ne_u64 vcc, s[82:83], v[228:229]\n"
"s_cbranch_vccnz SLK%=\n"
"BKK%=:\n"
"E 178,178\n"
"Gvaa 144,68,208\n"
"s_add_u32 s54, s54, 4096\n"
"buffer_load_dwordx4 a[112:115], v232, s[20:23], s54 offen sc1\n"
"E 179,179\n"
"Svav 0,16,208,254\n"
"s_add_u32 s15, s15, 1\n"
"ds_read_b128 a[4:7], v224 offset:25520\n"
"ds_read_b32 a24, v227 offset:27840\n"
"E 180,180\n"
"E 181,181\n"
"Gzvaa 148,68,212\n"
"s_and_b32 s15, s15, 7\n"
"E 182,182\n"
"Gvaa 148,64,196\n"
"s_mul_i32 s60, s15, 18432\n"
"E 183,183\n"
"Mvaa 192,0,56\n"
"s_add_u32 s60, s60, s66\n"
"Ta 80,239,18624\n"
"Ta 82,239,23232\n"
"E 184,184\n"
"E 185,185\n"
"Gzvaa 152,64,200\n"
"buffer_load_dwordx4 a[36:39], v232, s[20:23], s60 offen sc1\n"
"E 186,186\n"
"Gvaa 152,68,216\n"
"E 187,187\n"
"Svav 0,20,216,254\n"
"Ta 84,239,27840\n"
"E 188,188\n"
"E 189,189\n"
"Gzvaa 156,68,220\n"
"E 190,190\n"
"Gvaa 156,64,204\n"
"WL 7\n"
"E 191,191\n"
"Nzvaa 208,26,32\n"
"Ta 86,239,32448\n"
"Ta 88,239,18656\n"
"E 192,192\n"
"E 193,193\n"
"Gzvaa 172,72,204\n"
"buffer_load_dwordx4 a[116:119], v232, s[20:23], s54 offen offset:1024 sc1\n"
"E 194,194\n"
"Gvaa 172,76,220\n"
"buffer_load_dwordx4 a[40:43], v232, s[20:23], s60 offen offset:1024 sc1\n"
"E 195,195\n"
"Svav 16,16,176,254\n"
"Ta 90,239,23264\n"
"Ta 92,239,27872\n"
"E 196,196\n"
"E 197,197\n"
"Gzvaa 168,76,216\n"
"E 198,198\n"
"Gvaa 168,72,200\n"
"E 199,199\n"
"Mvaa 208,0,60\n"
"Ta 94,239,32480\n"
"buffer_load_dwordx4 a[44:47], v232, s[20:23], s60 offen offset:2304 sc1\n"
"E 200,200\n"
"E 201,201\n"
"Gzvaa 164,72,196\n"
"E 202,202\n"
"Gvaa 164,76,212\n"
"E 203,203\n"
"Svav 16,20,184,254\n"
"buffer_load_dwordx4 a[120:123], v232, s[20:23], s54 offen offset:2048 sc1\n"
"E 204,204\n"
"E 205,205\n"
"Gzvaa 160,76,208\n"
"E 206,206\n"
"Gvaa 160,72,192\n"
"WL 4\n"
"E 207,207\n"
"Nzvaa 176,26,30\n"
"ds_read_b128 a[16:19], v225 offset:25520\n"
"ds_read_b128 a[20:23], v226 offset:25520\n"
"E 208,208\n"
"E 209,209\n"
"Gzvaa 128,80,192\n"
"buffer_load_dwordx4 a[48:51], v232, s[20:23], s60 offen offset:3328 sc1\n"
"E 210,210\n"
"Gvaa 128,84,208\n"
"E 211,211\n"
"Svav 0,8,192,254\n"
"ds_read_b128 a[0:3], v224 offset:27840\n"
"Ta 64,239,18688\n"
"E 212,212\n"
"E 213,213\n"
"Gzvaa 132,84,212\n"
"E 214,214\n"
"Gvaa 132,80,196\n"
"E 215,215\n"
"Mvaa 176,4,56\n"
"Ta 66,239,23296\n"
"Ta 68,239,27904\n"
"E 216,216\n"
"E 217,217\n"
"Gzvaa 136,80,200\n"
"buffer_load_dwordx4 a[124:127], v232, s[20:23], s54 offen offset:3072 sc1\n"
"E 218,218\n"
"Gvaa 136,84,216\n"
"s_add_u32 s54, s52, 3\n"
"buffer_load_dwordx4 a[52:55], v250, s[20:23], s60 offen sc1\n"
"E 219,219\n"
"Svav 0,12,200,254\n"
"s_and_b32 s54, s54, 3\n"
"Ta 70,239,32512\n"
"E 220,220\n"
"E 221,221\n"
"Gzvaa 140,84,220\n"
"s_lshl_b32 s54, s54, 13\n"
"E 222,222\n"
"Gvaa 140,80,204\n"
"WL 7\n"
"E 223,223\n"
"Nzvaa 192,26,34\n"
"s_add_u32 s67, s67, 73728\n"
"Ta 72,239,0\n"
"Ta 74,239,4608\n"
"E 176,176\n"
"E 177,177\n"
"Gzvaa 124,88,204\n"
"s_add_u32 s53, s52, 0\n"
"E 178,178\n"
"Gvaa 124,92,220\n"
"s_and_b32 s53, s53, 3\n"
"E 179,179\n"
"Svav 16,8,208,254\n"
"s_mul_i32 s53, s53, 147456\n"
"Ta 76,239,9216\n"
"Ta 78,239,13824\n"
"E 180,180\n"
"E 181,181\n"
"Gzvaa 120,92,216\n"
"s_add_u32 s53, s53, s67\n"
"E 182,182\n"
"Gvaa 120,88,200\n"
"s_add_u32 m0, s84, 36864\n"
"E 183,183\n"
"Mvaa 192,4,60\n"
"s_add_u32 s54, s54, s64\n"
"L 240,36,53\n"
"buffer_load_dwordx4 a[128:131], v232, s[20:23], s54 offen sc1\n"
"E 184,184\n"
"E 185,185\n"
"Gzvaa 116,88,196\n"
"E 186,186\n"
"Gvaa 116,92,212\n"
"E 187,187\n"
"Svav 16,12,216,254\n"
"buffer_load_dwordx4 a[132:135], v232, s[20:23], s54 offen offset:1024 sc1\n"
"E 188,188\n"
"E 189,189\n"
"Gzvaa 112,92,208\n"
"E 190,190\n"
"Gvaa 112,88,192\n"
"WL 4\n"
"E 191,191\n"
"Nzvaa 208,24,28\n"
"s_add_u32 m0, s84, 37888\n"
"ds_read_b128 a[8:11], v225 offset:27840\n"
"ds_read_b128 a[12:15], v226 offset:27840\n"
"E 192,192\n"
"E 193,193\n"
"Gzvaa 80,64,192\n"
"L 241,36,53\n"
"E 194,194\n"
"Gvaa 80,68,208\n"
"E 195,195\n"
"Svav 0,16,176,254\n"
"ds_read_b128 a[4:7], v224 offset:30160\n"
"ds_read_b32 a26, v227 offset:32480\n"
"E 196,196\n"
"E 197,197\n"
"Gzvaa 84,68,212\n"
"E 198,198\n"
"Gvaa 84,64,196\n"
"E 199,199\n"
"Mvaa 208,0,56\n"
"Ta 80,239,32\n"
"Ta 82,239,4640\n"
"E 200,200\n"
"E 201,201\n"
"Gzvaa 88,64,200\n"
"buffer_load_dwordx4 a[136:139], v232, s[20:23], s54 offen offset:2048 sc1\n"
"E 202,202\n"
"Gvaa 88,68,216\n"
"E 203,203\n"
"Svav 0,20,184,254\n"
"Ta 84,239,9248\n"
"E 204,204\n"
"E 205,205\n"
"Gzvaa 92,68,220\n"
"E 206,206\n"
"Gvaa 92,64,204\n"
"WL 7\n"
"E 207,207\n"
"Nzvaa 176,24,32\n"
"WV 19\n"
"Ta 86,239,13856\n"
"Ta 88,239,64\n"
"E 208,208\n"
"E 209,209\n"
"Gvaa 44,72,236\n"
"s_add_u32 m0, s84, 38912\n"
"E 210,210\n"
"Gvaa 44,76,252\n"
"L 242,36,53\n"
"E 211,211\n"
"Svav 16,16,192,254\n"
"Ta 90,239,4672\n"
"Ta 92,239,9280\n"
"E 212,212\n"
"E 213,213\n"
"Gvaa 40,76,248\n"
"E 214,214\n"
"Gvaa 40,72,232\n"
"buffer_load_dwordx4 a[140:143], v232, s[20:23], s54 offen offset:3072 sc1\n"
"E 215,215\n"
"Mvaa 176,0,60\n"
"Ta 94,239,13888\n"
"E 216,216\n"
"E 217,217\n"
"Gvaa 36,72,228\n"
"E 218,218\n"
"Gvaa 36,76,244\n"
"E 219,219\n"
"Svav 16,20,200,254\n"
"E 220,220\n"
"E 221,221\n"
"Gvaa 32,76,240\n"
"E 222,222\n"
"Gvaa 32,72,224\n"
"WL 4\n"
"E 223,223\n"
"Nzvaa 192,24,30\n"
"s_add_u32 m0, s84, 39936\n"
"ds_read_b128 a[16:19], v225 offset:30160\n"
"ds_read_b128 a[20:23], v226 offset:30160\n"
"E 176,176\n"
"E 177,177\n"
"Gvaa 48,80,224\n"
"E 178,178\n"
"Gvaa 48,84,240\n"
"s_add_u32 s54, s54, 4096\n"
"L 243,36,53\n"
"E 179,179\n"
"Svav 0,8,208,254\n"
"ds_read_b128 a[0:3], v224 offset:32480\n"
"Ta 64,239,96\n"
"E 180,180\n"
"E 181,181\n"
"Gvaa 52,84,244\n"
"E 182,182\n"
"Gvaa 52,80,228\n"
"buffer_load_dwordx4 a[144:147], v232, s[20:23], s54 offen sc1\n"
"E 183,183\n"
"Mvaa 192,4,56\n"
"WV 16\n"
"Ta 66,239,4704\n"
"Ta 68,239,9312\n"
"E 184,184\n"
"E 185,185\n"
"Gvaa 56,80,232\n"
"E 186,186\n"
"Gvaa 56,84,248\n"
"ds_write_b128 v244, a[36:39] offset:37120\n"
"E 187,187\n"
"Svav 0,12,216,254\n"
"Ta 70,239,13920\n"
"E 188,188\n"
"E 189,189\n"
"Gvaa 60,84,252\n"
"E 190,190\n"
"Gvaa 60,80,236\n"
"WL 8\n"
"E 191,191\n"
"Nzvaa 208,24,34\n"
"s_add_u32 m0, s84, 40960\n"
"Ta 72,239,128\n"
"Ta 74,239,4736\n"
"E 192,192\n"
"E 193,193\n"
"Gvaa 76,88,236\n"
"WV 14\n"
"E 194,194\n"
"Gvaa 76,92,252\n"
"s_mov_b32 exec_hi, 0\n"
"L 238,36,53\n"
"s_mov_b32 exec_hi, -1\n"
"E 195,195\n"
"Svav 16,8,176,254\n"
"Ta 76,239,9344\n"
"Ta 78,239,13952\n"
"E 196,196\n"
"E 197,197\n"
"Gvaa 72,92,248\n"
"E 198,198\n"
"Gvaa 72,88,232\n"
"buffer_load_dwordx4 a[148:151], v232, s[20:23], s54 offen offset:1024 sc1\n"
"E 199,199\n"
"Mvaa 208,4,60\n"
"ds_write_b128 v244, a[40:43] offset:38144\n"
"E 200,200\n"
"E 201,201\n"
"Gvaa 68,88,228\n"
"E 202,202\n"
"Gvaa 68,92,244\n"
"WV 15\n"
"ds_write_b128 v244, a[44:47] offset:39440\n"
"E 203,203\n"
"Svav 16,12,184,254\n"
"buffer_load_dwordx4 a[152:155], v232, s[20:23], s54 offen offset:2048 sc1\n"
"E 204,204\n"
"E 205,205\n"
"Gvaa 64,92,240\n"
"E 206,206\n"
"Gvaa 64,88,224\n"
"WL 6\n"
"E 207,207\n"
"Nzvaa 176,26,28\n"
"ds_read_b128 a[8:11], v225 offset:32480\n"
"ds_read_b128 a[12:15], v226 offset:32480\n"
"E 208,208\n"
"E 209,209\n"
"Gvaa 96,64,224\n"
"E 210,210\n"
"Gvaa 96,68,240\n"
"buffer_load_dwordx2 v[228:229], v253, s[20:23], s80 offen offset:32 sc1\n"
"E 211,211\n"
"Svav 0,16,192,254\n"
"WV 15\n"
"ds_read_b128 a[4:7], v224 offset:34800\n"
"Ta 80,239,160\n"
"E 212,212\n"
"E 213,213\n"
"Gvaa 100,68,244\n"
"E 214,214\n"
"Gvaa 100,64,228\n"
"ds_write_b128 v244, a[48:51] offset:40464\n"
"E 215,215\n"
"Mvaa 176,0,56\n"
"Ta 82,239,4768\n"
"Ta 84,239,9376\n"
"E 216,216\n"
"E 217,217\n"
"Gvaa 104,64,232\n"
"E 218,218\n"
"Gvaa 104,68,248\n"
"buffer_load_dwordx4 a[156:159], v232, s[20:23], s54 offen offset:3072 sc1\n"
"E 219,219\n"
"Svav 0,20,200,254\n"
"Ta 86,239,13984\n"
"E 220,220\n"
"E 221,221\n"
"Gvaa 108,68,252\n"
"WV 14\n"
"E 222,222\n"
"Gvaa 108,64,236\n"
"WL 10\n"
"ds_write_b128 v245, a[52:55] offset:37120\n"
"E 223,223\n"
"Nzvaa 192,26,32\n"
"s_add_u32 s67, s67, 4294893568\n"
"Ta 88,239,192\n"
"Ta 90,239,4800\n"
"E 176,176\n"
"E 177,177\n"
"Gvaa 156,72,236\n"
"s_add_u32 s53, s52, 3\n"
"E 178,178\n"
"Gvaa 156,76,252\n"
"s_and_b32 s53, s53, 3\n"
"E 179,179\n"
"Svav 16,16,208,254\n"
"s_mul_i32 s53, s53, 147456\n"
"Ta 92,239,9408\n"
"Ta 94,239,14016\n"
"E 180,180\n"
"E 181,181\n"
"Gvaa 152,76,248\n"
"s_add_u32 s53, s53, s67\n"
"E 182,182\n"
"Gvaa 152,72,232\n"
"s_add_u32 m0, s84, 73728\n"
"E 183,183\n"
"Mvaa 192,0,60\n"
"s_add_u32 s15, s15, 1\n"
"L 240,36,53\n"
"E 184,184\n"
"E 185,185\n"
"Gvaa 148,72,228\n"
"s_and_b32 s15, s15, 7\n"
"E 186,186\n"
"Gvaa 148,76,244\n"
"s_mul_i32 s60, s15, 18432\n"
"E 187,187\n"
"Svav 16,20,216,254\n"
"s_add_u32 s60, s60, s66\n"
"E 188,188\n"
"E 189,189\n"
"Gvaa 144,76,240\n"
"E 190,190\n"
"Gvaa 144,72,224\n"
"WL 4\n"
"E 191,191\n"
"Nzvaa 208,26,30\n"
"s_add_u32 m0, s84, 74752\n"
"s_barrier\n"
"ds_read_b128 a[16:19], v225 offset:34800\n"
"E 192,192\n"
"E 193,193\n"
"Gvaa 160,80,224\n"
"E 194,194\n"
"Gvaa 160,84,240\n"
"L 241,36,53\n"
"E 195,195\n"
"Svav 0,8,176,254\n"
"ds_read_b128 a[20:23], v226 offset:34800\n"
"ds_read_b32 a24, v227 offset:37120\n"
"E 196,196\n"
"E 197,197\n"
"Gvaa 164,84,244\n"
"E 198,198\n"
"Gvaa 164,80,228\n"
"buffer_load_dwordx4 a[36:39], v232, s[20:23], s60 offen sc1\n"
"E 199,199\n"
"Mvaa 208,4,56\n"
"ds_read_b128 a[0:3], v224 offset:37120\n"
"Ta 64,239,224\n"
"E 200,200\n"
"E 201,201\n"
"Gvaa 168,80,232\n"
"E 202,202\n"
"Gvaa 168,84,248\n"
"buffer_load_dwordx4 a[40:43], v232, s[20:23], s60 offen offset:1024 sc1\n"
"E 203,203\n"
"Svav 0,12,184,254\n"
"Ta 66,239,4832\n"
"E 204,204\n"
"E 205,205\n"
"Gvaa 172,84,252\n"
"E 206,206\n"
"Gvaa 172,80,236\n"
"WL 6\n"
"E 207,207\n"
"Nzvaa 176,26,34\n"
"s_add_u32 m0, s84, 75776\n"
"Ta 68,239,9440\n"
"Ta 70,239,14048\n"
"E 208,208\n"
"E 209,209\n"
"Gvaa 140,88,236\n"
"WV 5\n"
"E 210,210\n"
"Gvaa 140,92,252\n"
"L 242,36,53\n"
"E 211,211\n"
"Svav 16,8,192,254\n"
"Ta 72,239,256\n"
"Ta 74,239,4864\n"
"E 212,212\n"
"E 213,213\n"
"Gvaa 136,92,248\n"
"E 214,214\n"
"Gvaa 136,88,232\n"
"v_cmp_ne_u64 vcc, s[82:83], v[228:229]\n"
"E 215,215\n"
"Mvaa 176,4,60\n"
"Ta 76,239,9472\n"
"Ta 78,239,14080\n"
"E 216,216\n"
"E 217,217\n"
"Gvaa 132,88,228\n"
"E 218,218\n"
"Gvaa 132,92,244\n"
"s_cbranch_vccnz SLI%=\n"
"BKI%=:\n"
"E 219,219\n"
"Svav 16,12,200,254\n"
"s_add_u32 s64, s64, 32768\n"
"buffer_load_dwordx4 a[44:47], v232, s[20:23], s60 offen offset:2304 sc1\n"
"buffer_load_dwordx4 a[48:51], v232, s[20:23], s60 offen offset:3328 sc1\n"
"E 220,220\n"
"E 221,221\n"
"Gvaa 128,92,240\n"
"s_add_u32 s54, s52, 0\n"
"E 222,222\n"
"Gvaa 128,88,224\n"
"WL 4\n"
"E 223,223\n"
"Nzvaa 192,24,28\n"
"s_add_u32 m0, s84, 76800\n"
"ds_read_b128 a[8:11], v225 offset:37120\n"
"ds_read_b128 a[12:15], v226 offset:37120\n"
"E 176,176\n"
"E 177,177\n"
"Gvaa 112,64,224\n"
"s_and_b32 s54, s54, 3\n"
"E 178,178\n"
"Gvaa 112,68,240\n"
"s_lshl_b32 s54, s54, 13\n"
"L 243,36,53\n"
"E 179,179\n"
"Svav 0,16,208,254\n"
"s_add_u32 s54, s54, s64\n"
"ds_read_b128 a[4:7], v224 offset:39440\n"
"ds_read_b32 a26, v227 offset:41760\n"
"E 180,180\n"
"E 181,181\n"
"Gvaa 116,68,244\n"
"E 182,182\n"
"Gvaa 116,64,228\n"
"buffer_load_dwordx4 a[192:195], v232, s[20:23], s54 offen sc1\n"
"E 183,183\n"
"Mvaa 192,0,56\n"
"Ta 80,236,0\n"
"Ta 82,236,4608\n"
"E 184,184\n"
"E 185,185\n"
"Gvaa 120,64,232\n"
"E 186,186\n"
"Gvaa 120,68,248\n"
"buffer_load_dwordx4 a[52:55], v250, s[20:23], s60 offen sc1\n"
"E 187,187\n"
"Svav 0,20,216,254\n"
"Ta 84,236,9216\n"
"buffer_load_dwordx4 a[196:199], v232, s[20:23], s54 offen offset:1024 sc1\n"
"E 188,188\n"
"E 189,189\n"
"Gvaa 124,68,252\n"
"E 190,190\n"
"Gvaa 124,64,236\n"
"WL 7\n"
"E 191,191\n"
"Nzvaa 208,24,32\n"
"s_add_u32 m0, s84, 77824\n"
"Ta 86,236,13824\n"
"Ta 88,236,32\n"
"E 192,192\n"
"E 193,193\n"
"Gvaa 92,72,236\n"
"E 194,194\n"
"Gvaa 92,76,252\n"
"s_mov_b32 exec_hi, 0\n"
"L 238,36,53\n"
"s_mov_b32 exec_hi, -1\n"
"E 195,195\n"
"Svav 16,16,176,254\n"
"Ta 90,236,4640\n"
"Ta 92,236,9248\n"
"E 196,196\n"
"E 197,197\n"
"Gvaa 88,76,248\n"
"E 198,198\n"
"Gvaa 88,72,232\n"
"E 199,199\n"
"Mvaa 208,0,60\n"
"Ta 94,236,13856\n"
"E 200,200\n"
"E 201,201\n"
"Gvaa 84,72,228\n"
"E 202,202\n"
"Gvaa 84,76,244\n"
"buffer_load_dwordx4 a[200:203], v232, s[20:23], s54 offen offset:2048 sc1\n"
"E 203,203\n"
"Svav 16,20,184,254\n"
"E 204,204\n"
"E 205,205\n"
"Gvaa 80,76,240\n"
"E 206,206\n"
"Gvaa 80,72,224\n"
"WL 4\n"
"E 207,207\n"
"Nzvaa 176,24,30\n"
"ds_read_b128 a[16:19], v225 offset:39440\n"
"ds_read_b128 a[20:23], v226 offset:39440\n"
"E 208,208\n"
"E 209,209\n"
"Gvaa 32,80,96\n"
"E 210,210\n"
"Gvaa 32,84,112\n"
"E 211,211\n"
"Svav 0,8,192,254\n"
"ds_read_b128 a[0:3], v224 offset:41760\n"
"Ta 64,236,64\n"
"E 212,212\n"
"E 213,213\n"
"Gvaa 36,84,116\n"
"E 214,214\n"
"Gvaa 36,80,100\n"
"buffer_load_dwordx4 a[204:207], v232, s[20:23], s54 offen offset:3072 sc1\n"
"E 215,215\n"
"Mvaa 176,4,56\n"
"Ta 66,236,4672\n"
"Ta 68,236,9280\n"
"E 216,216\n"
"E 217,217\n"
"Gvaa 40,80,104\n"
"E 218,218\n"
"Gvaa 40,84,120\n"
"E 219,219\n"
"Svav 0,12,200,254\n"
"Ta 70,236,13888\n"
"E 220,220\n"
"E 221,221\n"
"Gvaa 44,84,124\n"
"E 222,222\n"
"Gvaa 44,80,108\n"
"WL 7\n"
"E 223,223\n"
"Nzvaa 192,24,34\n"
"Ta 72,236,96\n"
"Ta 74,236,4704\n"
"E 176,176\n"
"E 177,177\n"
"Gvaa 60,88,108\n"
"E 178,178\n"
"Gvaa 60,92,124\n"
"s_add_u32 s54, s54, 4096\n"
"buffer_load_dwordx4 a[208:211], v232, s[20:23], s54 offen sc1\n"
"E 179,179\n"
"Svav 16,8,208,254\n"
"Ta 76,236,9312\n"
"Ta 78,236,13920\n"
"E 180,180\n"
"E 181,181\n"
"Gvaa 56,92,120\n"
"E 182,182\n"
"Gvaa 56,88,104\n"
"E 183,183\n"
"Mvaa 192,4,60\n"
"E 184,184\n"
"E 185,185\n"
"Gvaa 52,88,100\n"
"E 186,186\n"
"Gvaa 52,92,116\n"
"E 187,187\n"
"Svav 16,12,216,254\n"
"E 188,188\n"
"E 189,189\n"
"Gvaa 48,92,112\n"
"E 190,190\n"
"Gvaa 48,88,96\n"
"WL 4\n"
"E 191,191\n"
"Nzvaa 208,26,28\n"
"WV 12\n"
"ds_read_b128 a[8:11], v225 offset:41760\n"
"ds_read_b128 a[12:15], v226 offset:41760\n"
"E 192,192\n"
"E 193,193\n"
"Gvaa 64,64,96\n"
"E 194,194\n"
"Gvaa 64,68,112\n"
"buffer_load_dwordx4 a[212:215], v232, s[20:23], s54 offen offset:1024 sc1\n"
"E 195,195\n"
"Svav 0,16,176,254\n"
"ds_read_b128 a[4:7], v224 offset:44080\n"
"ds_read_b32 a24, v227 offset:46400\n"
"E 196,196\n"
"E 197,197\n"
"Gvaa 68,68,116\n"
"E 198,198\n"
"Gvaa 68,64,100\n"
"ds_write_b128 v244, a[36:39] offset:0\n"
"E 199,199\n"
"Mvaa 208,0,56\n"
"Ta 80,236,128\n"
"Ta 82,236,4736\n"
"E 200,200\n"
"E 201,201\n"
"Gvaa 72,64,104\n"
"WV 12\n"
"E 202,202\n"
"Gvaa 72,68,120\n"
"ds_write_b128 v244, a[40:43] offset:1024\n"
"E 203,203\n"
"Svav 0,20,184,254\n"
"Ta 84,236,9344\n"
"buffer_load_dwordx4 a[216:219], v232, s[20:23], s54 offen offset:2048 sc1\n"
"E 204,204\n"
"E 205,205\n"
"Gvaa 76,68,124\n"
"E 206,206\n"
"Gvaa 76,64,108\n"
"WL 9\n"
"ds_read_b32 v237, v234 offset:9228\n"
"E 207,207\n"
"Nzvaa 176,26,32\n"
"Ta 86,236,13952\n"
"Ta 88,236,160\n"
"E 208,208\n"
"E 209,209\n"
"Gvaa 108,72,108\n"
"E 210,210\n"
"Gvaa 108,76,124\n"
"WV 11\n"
"ds_write_b128 v244, a[44:47] offset:2320\n"
"E 211,211\n"
"Svav 16,16,192,254\n"
"Ta 90,236,4768\n"
"Ta 92,236,9376\n"
"E 212,212\n"
"E 213,213\n"
"Gvaa 104,76,120\n"
"E 214,214\n"
"Gvaa 104,72,104\n"
"ds_read_b128 v[246:249], v234 offset:8192\n"
"E 215,215\n"
"Mvaa 176,0,60\n"
"WL 0\n"
"Ta 94,236,13984\n"
"v_add_u32 v237, s85, v237\n"
"E 216,216\n"
"E 217,217\n"
"Gvaa 100,72,100\n"
"s_and_b32 s93, s55, 1\n"
"E 218,218\n"
"Gvaa 100,76,116\n"
"s_add_u32 s93, s93, -1\n"
"buffer_load_dwordx4 a[220:223], v232, s[20:23], s54 offen offset:3072 sc1\n"
"E 219,219\n"
"Svav 16,20,200,254\n"
"s_add_u32 s54, s52, 1\n"
"E 220,220\n"
"E 221,221\n"
"Gvaa 96,76,112\n"
"s_and_b32 s54, s54, 3\n"
"E 222,222\n"
"Gvaa 96,72,96\n"
"WV 11\n"
"ds_write_b128 v244, a[48:51] offset:3344\n"
"E 223,223\n"
"Nzvaa 192,26,30\n"
"s_lshl_b32 s54, s54, 13\n"
"ds_read_b128 a[16:19], v225 offset:44080\n"
"ds_read_b128 a[20:23], v226 offset:44080\n"
"E 176,176\n"
"E 177,177\n"
"Gvaa 144,80,96\n"
"s_add_u32 s54, s54, s64\n"
"E 178,178\n"
"Gvaa 144,84,112\n"
"v_accvgpr_write_b32 a182, 0\n"
"E 179,179\n"
"Svav 0,8,208,254\n"
"ds_read_b128 a[0:3], v224 offset:46400\n"
"Ta 64,236,192\n"
"E 180,180\n"
"E 181,181\n"
"Gvaa 148,84,116\n"
"WV 8\n"
"E 182,182\n"
"Gvaa 148,80,100\n"
"ds_write_b128 v245, a[52:55] offset:0\n"
"E 183,183\n"
"Mvaa 192,4,56\n"
"s_add_u32 s15, s15, 1\n"
"Ta 66,236,4800\n"
"Ta 68,236,9408\n"
"E 184,184\n"
"E 185,185\n"
"Gvaa 152,80,104\n"
"s_and_b32 s15, s15, 7\n"
"E 186,186\n"
"Gvaa 152,84,120\n"
"s_mul_i32 s60, s15, 18432\n"
"E 187,187\n"
"Svav 0,12,216,254\n"
"s_add_u32 s60, s60, s66\n"
"Ta 70,236,14016\n"
"E 188,188\n"
"E 189,189\n"
"Gvaa 156,84,124\n"
"E 190,190\n"
"Gvaa 156,80,108\n"
"WL 9\n"
"E 191,191\n"
"Nzvaa 208,26,34\n"
"Ta 72,236,224\n"
"Ta 74,236,4832\n"
"E 192,192\n"
"E 193,193\n"
"Gvaa 172,88,108\n"
"E 194,194\n"
"Gvaa 172,92,124\n"
"buffer_load_dwordx4 a[224:227], v232, s[20:23], s54 offen sc1\n"
"E 195,195\n"
"Svav 16,8,176,254\n"
"Ta 76,236,9440\n"
"Ta 78,236,14048\n"
"E 196,196\n"
"E 197,197\n"
"Gvaa 168,92,120\n"
"E 198,198\n"
"Gvaa 168,88,104\n"
"v_accvgpr_write_b32 a183, 0\n"
"E 199,199\n"
"Mvaa 208,4,60\n"
"E 200,200\n"
"E 201,201\n"
"Gvaa 164,88,100\n"
"E 202,202\n"
"Gvaa 164,92,116\n"
"E 203,203\n"
"Svav 16,12,184,254\n"
"buffer_load_dwordx4 a[228:231], v232, s[20:23], s54 offen offset:1024 sc1\n"
"E 204,204\n"
"E 205,205\n"
"Gvaa 160,92,112\n"
"E 206,206\n"
"Gvaa 160,88,96\n"
"s_waitcnt vmcnt(8) lgkmcnt(4)\n"
"E 207,207\n"
"Nzvaa 176,24,28\n"
"s_barrier\n"
"ds_read_b128 a[8:11], v225 offset:46400\n"
"E 208,208\n"
"E 209,209\n"
"Gvaa 128,64,96\n"
"E 210,210\n"
"Gvaa 128,68,112\n"
"v_accvgpr_write_b32 a186, 0\n"
"E 211,211\n"
"Svav 0,16,192,254\n"
"ds_read_b128 a[12:15], v226 offset:46400\n"
"ds_read_b128 a[4:7], v224 offset:48720\n"
"E 212,212\n"
"E 213,213\n"
"Gvaa 132,68,116\n"
"E 214,214\n"
"Gvaa 132,64,100\n"
"E 215,215\n"
"Mvaa 176,0,56\n"
"ds_read_b32 a26, v227 offset:51040\n"
"Ta 80,236,256\n"
"E 216,216\n"
"E 217,217\n"
"Gvaa 136,64,104\n"
"E 218,218\n"
"Gvaa 136,68,120\n"
"buffer_load_dwordx4 a[232:235], v232, s[20:23], s54 offen offset:2048 sc1\n"
"E 219,219\n"
"Svav 0,20,200,254\n"
"Ta 82,236,4864\n"
"E 220,220\n"
"E 221,221\n"
"Gvaa 140,68,124\n"
"E 222,222\n"
"Gvaa 140,64,108\n"
"WL 6\n"
"E 223,223\n"
"Nzvaa 192,24,32\n"
"s_add_u32 s67, s67, 73728\n"
"Ta 84,236,9472\n"
"Ta 86,236,14080\n"
"E 176,176\n"
"E 177,177\n"
"Gvaa 124,72,108\n"
"s_add_u32 s53, s52, 1\n"
"E 178,178\n"
"Gvaa 124,76,124\n"
"s_and_b32 s53, s53, 3\n"
"buffer_load_dwordx4 a[36:39], v232, s[20:23], s60 offen sc1\n"
"E 179,179\n"
"Svav 16,16,208,254\n"
"s_mul_i32 s53, s53, 147456\n"
"Ta 88,236,18432\n"
"Ta 90,236,23040\n"
"E 180,180\n"
"E 181,181\n"
"Gvaa 120,76,120\n"
"s_add_u32 s53, s53, s67\n"
"E 182,182\n"
"Gvaa 120,72,104\n"
"s_add_u32 m0, s84, 18432\n"
"buffer_load_dwordx4 a[236:239], v232, s[20:23], s54 offen offset:3072 sc1\n"
"E 183,183\n"
"Mvaa 192,0,60\n"
"Ta 92,236,27648\n"
"Ta 94,236,32256\n"
"E 184,184\n"
"E 185,185\n"
"Gvaa 116,72,100\n"
"E 186,186\n"
"Gvaa 116,76,116\n"
"L 240,36,53\n"
"E 187,187\n"
"Svav 16,20,216,254\n"
"buffer_load_dwordx4 a[40:43], v232, s[20:23], s60 offen offset:1024 sc1\n"
"v_accvgpr_write_b32 a187, 0\n"
"E 188,188\n"
"E 189,189\n"
"Gvaa 112,76,112\n"
"E 190,190\n"
"Gvaa 112,72,96\n"
"WL 4\n"
"E 191,191\n"
"Nzvaa 208,24,30\n"
"s_add_u32 m0, s84, 19456\n"
"ds_read_b128 a[16:19], v225 offset:48720\n"
"ds_read_b128 a[20:23], v226 offset:48720\n"
"E 192,192\n"
"E 193,193\n"
"Gvaa 80,80,96\n"
"E 194,194\n"
"Gvaa 80,84,112\n"
"s_add_u32 s54, s54, 4096\n"
"L 241,36,53\n"
"E 195,195\n"
"Svav 0,8,176,254\n"
"ds_read_b128 a[0:3], v224 offset:51040\n"
"Ta 64,236,18464\n"
"E 196,196\n"
"E 197,197\n"
"Gvaa 84,84,116\n"
"E 198,198\n"
"Gvaa 84,80,100\n"
"buffer_load_dwordx4 a[240:243], v232, s[20:23], s54 offen sc1\n"
"E 199,199\n"
"Mvaa 208,4,56\n"
"Ta 66,236,23072\n"
"Ta 68,236,27680\n"
"E 200,200\n"
"E 201,201\n"
"Gvaa 88,80,104\n"
"E 202,202\n"
"Gvaa 88,84,120\n"
"buffer_load_dwordx4 a[44:47], v232, s[20:23], s60 offen offset:2304 sc1\n"
"E 203,203\n"
"Svav 0,12,184,254\n"
"Ta 70,236,32288\n"
"buffer_load_dwordx4 a[48:51], v232, s[20:23], s60 offen offset:3328 sc1\n"
"E 204,204\n"
"E 205,205\n"
"Gvaa 92,84,124\n"
"E 206,206\n"
"Gvaa 92,80,108\n"
"WL 7\n"
"v_accvgpr_write_b32 a184, v247\n"
"E 207,207\n"
"Nzvaa 176,24,34\n"
"Ta 72,236,18496\n"
"Ta 74,236,23104\n"
"E 208,208\n"
"E 209,209\n"
"Gvaa 44,88,140\n"
"s_add_u32 m0, s84, 20480\n"
"E 210,210\n"
"Gvaa 44,92,156\n"
"L 242,36,53\n"
"E 211,211\n"
"Svav 16,8,192,254\n"
"Ta 76,236,27712\n"
"Ta 78,236,32320\n"
"E 212,212\n"
"E 213,213\n"
"Gvaa 40,92,152\n"
"E 214,214\n"
"Gvaa 40,88,136\n"
"buffer_load_dwordx4 a[244:247], v232, s[20:23], s54 offen offset:1024 sc1\n"
"E 215,215\n"
"Mvaa 176,4,60\n"
"buffer_load_dwordx4 a[52:55], v250, s[20:23], s60 offen sc1\n"
"v_accvgpr_write_b32 a185, v248\n"
"E 216,216\n"
"E 217,217\n"
"Gvaa 36,88,132\n"
"E 218,218\n"
"Gvaa 36,92,148\n"
"E 219,219\n"
"Svav 16,12,200,254\n"
"buffer_load_dwordx4 a[248:251], v232, s[20:23], s54 offen offset:2048 sc1\n"
"E 220,220\n"
"E 221,221\n"
"Gvaa 32,92,144\n"
"E 222,222\n"
"Gvaa 32,88,128\n"
"WL 4\n"
"E 223,223\n"
"Nzvaa 192,26,28\n"
"s_add_u32 m0, s84, 21504\n"
"ds_read_b128 a[8:11], v225 offset:51040\n"
"ds_read_b128 a[12:15], v226 offset:51040\n"
"E 176,176\n"
"E 177,177\n"
"Gvaa 48,64,128\n"
"E 178,178\n"
"Gvaa 48,68,144\n"
"L 243,36,53\n"
"E 179,179\n"
"Svav 0,16,208,254\n"
"ds_read_b128 a[4:7], v224 offset:53360\n"
"ds_read_b32 a24, v227 offset:0\n"
"E 180,180\n"
"E 181,181\n"
"Gvaa 52,68,148\n"
"E 182,182\n"
"Gvaa 52,64,132\n"
"v_and_b32 v251, s93, v247\n"
"E 183,183\n"
"Mvaa 192,0,56\n"
"Ta 80,236,18528\n"
"Ta 82,236,23136\n"
"E 184,184\n"
"E 185,185\n"
"Gvaa 56,64,136\n"
"E 186,186\n"
"Gvaa 56,68,152\n"
"buffer_load_dwordx4 a[252:255], v232, s[20:23], s54 offen offset:3072 sc1\n"
"E 187,187\n"
"Svav 0,20,216,254\n"
"s_add_u32 s54, s52, 2\n"
"Ta 84,236,27744\n"
"E 188,188\n"
"E 189,189\n"
"Gvaa 60,68,156\n"
"s_and_b32 s54, s54, 3\n"
"E 190,190\n"
"Gvaa 60,64,140\n"
"WL 7\n"
"E 191,191\n"
"Nzvaa 208,26,32\n"
"s_add_u32 m0, s84, 22528\n"
"Ta 86,236,32352\n"
"Ta 88,236,18560\n"
"E 192,192\n"
"E 193,193\n"
"Gvaa 76,72,140\n"
"s_lshl_b32 s54, s54, 13\n"
"E 194,194\n"
"Gvaa 76,76,156\n"
"s_mov_b32 exec_hi, 0\n"
"L 238,36,53\n"
"s_mov_b32 exec_hi, -1\n"
"E 195,195\n"
"Svav 16,16,176,254\n"
"s_add_u32 s54, s54, s64\n"
"Ta 90,236,23168\n"
"Ta 92,236,27776\n"
"E 196,196\n"
"E 197,197\n"
"Gvaa 72,76,152\n"
"E 198,198\n"
"Gvaa 72,72,136\n"
"v_accvgpr_write_b32 a188, v251\n"
"E 199,199\n"
"Mvaa 208,0,60\n"
"Ta 94,236,32384\n"
"E 200,200\n"
"E 201,201\n"
"Gvaa 68,72,132\n"
"E 202,202\n"
"Gvaa 68,76,148\n"
"E 203,203\n"
"Svav 16,20,184,254\n"
"E 204,204\n"
"E 205,205\n"
"Gvaa 64,76,144\n"
"E 206,206\n"
"Gvaa 64,72,128\n"
"WL 4\n"
"E 207,207\n"
"Nzvaa 176,26,30\n"
"ds_read_b128 a[16:19], v225 offset:53360\n"
"ds_read_b128 a[20:23], v226 offset:53360\n"
"E 208,208\n"
"E 209,209\n"
"Gvaa 96,80,128\n"
"E 210,210\n"
"Gvaa 96,84,144\n"
"v_xor_b32 v251, v251, v247\n"
"E 211,211\n"
"Svav 0,8,192,254\n"
"ds_read_b128 a[0:3], v224 offset:0\n"
"Ta 64,236,18592\n"
"E 212,212\n"
"E 213,213\n"
"Gvaa 100,84,148\n"
"E 214,214\n"
"Gvaa 100,80,132\n"
"E 215,215\n"
"Mvaa 176,4,56\n"
"Ta 66,236,23200\n"
"Ta 68,236,27808\n"
"E 216,216\n"
"E 217,217\n"
"Gvaa 104,80,136\n"
"E 218,218\n"
"Gvaa 104,84,152\n"
"E 219,219\n"
"Svav 0,12,200,254\n"
"Ta 70,236,32416\n"
"E 220,220\n"
"E 221,221\n"
"Gvaa 108,84,156\n"
"E 222,222\n"
"Gvaa 108,80,140\n"
"WL 7\n"
"E 223,223\n"
"Nzvaa 192,26,34\n"
"s_add_u32 s53, s52, 2\n"
"Ta 72,236,18624\n"
"Ta 74,236,23232\n"
"E 176,176\n"
"E 177,177\n"
"Gvaa 156,88,140\n"
"s_and_b32 s53, s53, 3\n"
"E 178,178\n"
"Gvaa 156,92,156\n"
"s_mul_i32 s53, s53, 147456\n"
"buffer_load_dwordx4 a[96:99], v232, s[20:23], s54 offen sc1\n"
"E 179,179\n"
"Svav 16,8,208,254\n"
"s_add_u32 s53, s53, s67\n"
"Ta 76,236,27840\n"
"Ta 78,236,32448\n"
"E 180,180\n"
"E 181,181\n"
"Gvaa 152,92,152\n"
"s_add_u32 m0, s84, 0\n"
"E 182,182\n"
"Gvaa 152,88,136\n"
"WV 15\n"
"L 240,36,53\n"
"E 183,183\n"
"Mvaa 192,4,60\n"
"ds_write_b128 v244, a[36:39] offset:18560\n"
"v_accvgpr_write_b32 a190, v251\n"
"E 184,184\n"
"E 185,185\n"
"Gvaa 148,88,132\n"
"WV 13\n"
"E 186,186\n"
"Gvaa 148,92,148\n"
"ds_write_b128 v244, a[40:43] offset:19584\n"
"E 187,187\n"
"Svav 16,12,216,254\n"
"buffer_load_dwordx4 a[100:103], v232, s[20:23], s54 offen offset:1024 sc1\n"
"ds_read_b32 v228, v234 offset:5132\n"
"E 188,188\n"
"E 189,189\n"
"Gvaa 144,92,144\n"
"E 190,190\n"
"Gvaa 144,88,128\n"
"WL 7\n"
"E 191,191\n"
"Nzvaa 208,24,28\n"
"s_add_u32 m0, s84, 1024\n"
"ds_read_b128 a[8:11], v225 offset:0\n"
"ds_read_b128 a[12:15], v226 offset:0\n"
"E 192,192\n"
"E 193,193\n"
"Gvaa 160,64,128\n"
"WL 2\n"
"E 194,194\n"
"Gvaa 160,68,144\n"
"WV 11\n"
"L 241,36,53\n"
"E 195,195\n"
"Svav 0,16,176,254\n"
"ds_read_b128 a[4:7], v224 offset:2320\n"
"ds_read_b32 a26, v227 offset:4640\n"
"E 196,196\n"
"E 197,197\n"
"Gvaa 164,68,148\n"
"E 198,198\n"
"Gvaa 164,64,132\n"
"ds_write_b128 v244, a[44:47] offset:20880\n"
"E 199,199\n"
"Mvaa 208,0,56\n"
"Ta 80,236,18656\n"
"Ta 82,236,23264\n"
"E 200,200\n"
"E 201,201\n"
"Gvaa 168,64,136\n"
"E 202,202\n"
"Gvaa 168,68,152\n"
"buffer_load_dwordx4 a[104:107], v232, s[20:23], s54 offen offset:2048 sc1\n"
"E 203,203\n"
"Svav 0,20,184,254\n"
"WV 12\n"
"Ta 84,236,27872\n"
"ds_write_b128 v244, a[48:51] offset:21904\n"
"E 204,204\n"
"E 205,205\n"
"Gvaa 172,68,156\n"
"E 206,206\n"
"Gvaa 172,64,140\n"
"v_and_b32 v251, s93, v248\n"
"E 207,207\n"
"Nzvaa 176,24,32\n"
"s_add_u32 m0, s84, 2048\n"
"Ta 86,236,32480\n"
"Ta 88,236,18688\n"
"E 208,208\n"
"E 209,209\n"
"Gvaa 140,72,140\n"
"E 210,210\n"
"Gvaa 140,76,156\n"
"L 242,36,53\n"
"E 211,211\n"
"Svav 16,16,192,254\n"
"Ta 90,236,23296\n"
"Ta 92,236,27904\n"
"E 212,212\n"
"E 213,213\n"
"Gvaa 136,76,152\n"
"WV 10\n"
"E 214,214\n"
"Gvaa 136,72,136\n"
"buffer_load_dwordx4 a[108:111], v232, s[20:23], s54 offen offset:3072 sc1\n"
"E 215,215\n"
"Mvaa 176,0,60\n"
"Ta 94,236,32512\n"
"ds_write_b128 v245, a[52:55] offset:18560\n"
"E 216,216\n"
"E 217,217\n"
"Gvaa 132,72,132\n"
"s_add_u32 s15, s15, 1\n"
"E 218,218\n"
"Gvaa 132,76,148\n"
"s_and_b32 s15, s15, 7\n"
"v_accvgpr_write_b32 a189, v251\n"
"E 219,219\n"
"Svav 16,20,200,254\n"
"s_mul_i32 s60, s15, 18432\n"
"v_add_u32 v228, s57, v228\n"
"E 220,220\n"
"E 221,221\n"
"Gvaa 128,76,144\n"
"s_add_u32 s60, s60, s66\n"
"E 222,222\n"
"Gvaa 128,72,128\n"
"WL 5\n"
"buffer_load_dword v229, v228, s[40:43], 0 offen\n"
"E 223,223\n"
"Nzvaa 192,24,30\n"
"s_add_u32 m0, s84, 3072\n"
"ds_read_b128 a[16:19], v225 offset:2320\n"
"ds_read_b128 a[20:23], v226 offset:2320\n"
"E 176,176\n"
"E 177,177\n"
"Gvaa 112,80,128\n"
"E 178,178\n"
"Gvaa 112,84,144\n"
"s_add_u32 s54, s54, 4096\n"
"L 243,36,53\n"
"E 179,179\n"
"Svav 0,8,208,254\n"
"ds_read_b128 a[0:3], v224 offset:4640\n"
"Ta 64,239,36864\n"
"E 180,180\n"
"E 181,181\n"
"Gvaa 116,84,148\n"
"E 182,182\n"
"Gvaa 116,80,132\n"
"buffer_load_dwordx4 a[112:115], v232, s[20:23], s54 offen sc1\n"
"E 183,183\n"
"Mvaa 192,4,56\n"
"Ta 66,239,41472\n"
"Ta 68,239,46080\n"
"E 184,184\n"
"E 185,185\n"
"Gvaa 120,80,136\n"
"E 186,186\n"
"Gvaa 120,84,152\n"
"v_xor_b32 v251, v251, v248\n"
"E 187,187\n"
"Svav 0,12,216,254\n"
"Ta 70,239,50688\n"
"E 188,188\n"
"E 189,189\n"
"Gvaa 124,84,156\n"
"E 190,190\n"
"Gvaa 124,80,140\n"
"s_waitcnt vmcnt(10) lgkmcnt(7)\n"
"E 191,191\n"
"Nzvaa 208,24,34\n"
"s_add_u32 m0, s84, 4096\n"
"s_barrier\n"
"Ta 72,239,36896\n"
"E 192,192\n"
"E 193,193\n"
"Gvaa 92,88,140\n"
"E 194,194\n"
"Gvaa 92,92,156\n"
"s_mov_b32 exec_hi, 0\n"
"L 238,36,53\n"
"s_mov_b32 exec_hi, -1\n"
"E 195,195\n"
"Svav 16,8,176,254\n"
"Ta 74,239,41504\n"
"Ta 76,239,46112\n"
"E 196,196\n"
"E 197,197\n"
"Gvaa 88,92,152\n"
"E 198,198\n"
"Gvaa 88,88,136\n"
"buffer_load_dwordx4 a[116:119], v232, s[20:23], s54 offen offset:1024 sc1\n"
"E 199,199\n"
"Mvaa 208,4,60\n"
"Ta 78,239,50720\n"
"v_accvgpr_write_b32 a191, v251\n"
"E 200,200\n"
"E 201,201\n"
"Gvaa 84,88,132\n"
"E 202,202\n"
"Gvaa 84,92,148\n"
"E 203,203\n"
"Svav 16,12,184,254\n"
"buffer_load_dwordx4 a[120:123], v232, s[20:23], s54 offen offset:2048 sc1\n"
"E 204,204\n"
"E 205,205\n"
"Gvaa 80,92,144\n"
"E 206,206\n"
"Gvaa 80,88,128\n"
"WL 4\n"
"E 207,207\n"
"Nzvaa 176,26,28\n"
"ds_read_b128 a[8:11], v225 offset:4640\n"
"ds_read_b128 a[12:15], v226 offset:4640\n"
"E 208,208\n"
"E 209,209\n"
"Gvaa 32,64,192\n"
"E 210,210\n"
"Gvaa 32,68,208\n"
"E 211,211\n"
"Svav 0,16,192,254\n"
"ds_read_b128 a[4:7], v224 offset:6960\n"
"ds_read_b32 a24, v227 offset:9280\n"
"E 212,212\n"
"E 213,213\n"
"Gvaa 36,68,212\n"
"E 214,214\n"
"Gvaa 36,64,196\n"
"E 215,215\n"
"Mvaa 176,0,56\n"
"Ta 80,239,36928\n"
"Ta 82,239,41536\n"
"E 216,216\n"
"E 217,217\n"
"Gvaa 40,64,200\n"
"E 218,218\n"
"Gvaa 40,68,216\n"
"buffer_load_dwordx4 a[124:127], v232, s[20:23], s54 offen offset:3072 sc1\n"
"E 219,219\n"
"Svav 0,20,200,254\n"
"Ta 84,239,46144\n"
"E 220,220\n"
"E 221,221\n"
"Gvaa 44,68,220\n"
"s_add_u32 s54, s52, 3\n"
"E 222,222\n"
"Gvaa 44,64,204\n"
"WL 7\n"
"E 223,223\n"
"Nzvaa 192,26,32\n"
"s_add_u32 s53, s52, 3\n"
"Ta 86,239,50752\n"
"Ta 88,239,36960\n"
"E 176,176\n"
"E 177,177\n"
"Gvaa 60,72,204\n"
"s_and_b32 s53, s53, 3\n"
"E 178,178\n"
"Gvaa 60,76,220\n"
"s_mul_i32 s53, s53, 147456\n"
"E 179,179\n"
"Svav 16,16,208,254\n"
"s_add_u32 s53, s53, s67\n"
"Ta 90,239,41568\n"
"Ta 92,239,46176\n"
"E 180,180\n"
"E 181,181\n"
"Gvaa 56,76,216\n"
"s_add_u32 m0, s84, 55296\n"
"E 182,182\n"
"Gvaa 56,72,200\n"
"s_and_b32 s54, s54, 3\n"
"L 240,36,53\n"
"E 183,183\n"
"Mvaa 192,0,60\n"
"s_lshl_b32 s54, s54, 13\n"
"Ta 94,239,50784\n"
"E 184,184\n"
"E 185,185\n"
"Gvaa 52,72,196\n"
"s_add_u32 s54, s54, s64\n"
"E 186,186\n"
"Gvaa 52,76,212\n"
"E 187,187\n"
"Svav 16,20,216,254\n"
"E 188,188\n"
"E 189,189\n"
"Gvaa 48,76,208\n"
"E 190,190\n"
"Gvaa 48,72,192\n"
"WL 4\n"
"E 191,191\n"
"Nzvaa 208,26,30\n"
"s_add_u32 m0, s84, 56320\n"
"ds_read_b128 a[16:19], v225 offset:6960\n"
"ds_read_b128 a[20:23], v226 offset:6960\n"
"E 192,192\n"
"E 193,193\n"
"Gvaa 64,80,192\n"
"E 194,194\n"
"Gvaa 64,84,208\n"
"L 241,36,53\n"
"E 195,195\n"
"Svav 0,8,176,254\n"
"ds_read_b128 a[0:3], v224 offset:9280\n"
"Ta 64,239,36992\n"
"E 196,196\n"
"E 197,197\n"
"Gvaa 68,84,212\n"
"E 198,198\n"
"Gvaa 68,80,196\n"
"buffer_load_dwordx4 a[128:131], v232, s[20:23], s54 offen sc1\n"
"E 199,199\n"
"Mvaa 208,4,56\n"
"Ta 66,239,41600\n"
"Ta 68,239,46208\n"
"E 200,200\n"
"E 201,201\n"
"Gvaa 72,80,200\n"
"E 202,202\n"
"Gvaa 72,84,216\n"
"E 203,203\n"
"Svav 0,12,184,254\n"
"Ta 70,239,50816\n"
"buffer_load_dwordx4 a[132:135], v232, s[20:23], s54 offen offset:1024 sc1\n"
"E 204,204\n"
"E 205,205\n"
"Gvaa 76,84,220\n"
"E 206,206\n"
"Gvaa 76,80,204\n"
"WL 7\n"
"E 207,207\n"
"Nzvaa 176,26,34\n"
"s_add_u32 m0, s84, 57344\n"
"Ta 72,239,37024\n"
"Ta 74,239,41632\n"
"E 208,208\n"
"E 209,209\n"
"Gvaa 108,88,204\n"
"E 210,210\n"
"Gvaa 108,92,220\n"
"L 242,36,53\n"
"E 211,211\n"
"Svav 16,8,192,254\n"
"Ta 76,239,46240\n"
"Ta 78,239,50848\n"
"E 212,212\n"
"E 213,213\n"
"Gvaa 104,92,216\n"
"E 214,214\n"
"Gvaa 104,88,200\n"
"buffer_load_dwordx4 a[36:39], v232, s[20:23], s60 offen sc1\n"
"E 215,215\n"
"Mvaa 176,4,60\n"
"buffer_load_dwordx4 a[40:43], v232, s[20:23], s60 offen offset:1024 sc1\n"
"E 216,216\n"
"E 217,217\n"
"Gvaa 100,88,196\n"
"E 218,218\n"
"Gvaa 100,92,212\n"
"buffer_load_dwordx4 a[136:139], v232, s[20:23], s54 offen offset:2048 sc1\n"
"E 219,219\n"
"Svav 16,12,200,254\n"
"E 220,220\n"
"E 221,221\n"
"Gvaa 96,92,208\n"
"E 222,222\n"
"Gvaa 96,88,192\n"
"WL 4\n"
"E 223,223\n"
"Nzvaa 192,24,28\n"
"s_add_u32 m0, s84, 58368\n"
"ds_read_b128 a[8:11], v225 offset:9280\n"
"ds_read_b128 a[12:15], v226 offset:9280\n"
"E 176,176\n"
"E 177,177\n"
"Gvaa 144,64,192\n"
"E 178,178\n"
"Gvaa 144,68,208\n"
"L 243,36,53\n"
"E 179,179\n"
"Svav 0,16,208,254\n"
"ds_read_b128 a[4:7], v224 offset:11600\n"
"ds_read_b32 a26, v227 offset:13920\n"
"E 180,180\n"
"E 181,181\n"
"Gvaa 148,68,212\n"
"E 182,182\n"
"Gvaa 148,64,196\n"
"buffer_load_dwordx4 a[140:143], v232, s[20:23], s54 offen offset:3072 sc1\n"
"E 183,183\n"
"Mvaa 192,0,56\n"
"Ta 80,239,37056\n"
"Ta 82,239,41664\n"
"E 184,184\n"
"E 185,185\n"
"Gvaa 152,64,200\n"
"E 186,186\n"
"Gvaa 152,68,216\n"
"buffer_load_dwordx4 a[44:47], v232, s[20:23], s60 offen offset:2304 sc1\n"
"E 187,187\n"
"Svav 0,20,216,254\n"
"Ta 84,239,46272\n"
"buffer_load_dwordx4 a[48:51], v232, s[20:23], s60 offen offset:3328 sc1\n"
"E 188,188\n"
"E 189,189\n"
"Gvaa 156,68,220\n"
"E 190,190\n"
"Gvaa 156,64,204\n"
"WL 7\n"
"E 191,191\n"
"Nzvaa 208,24,32\n"
"s_add_u32 m0, s84, 59392\n"
"Ta 86,239,50880\n"
"Ta 88,239,37088\n"
"E 192,192\n"
"E 193,193\n"
"Gvaa 172,72,204\n"
"E 194,194\n"
"Gvaa 172,76,220\n"
"s_mov_b32 exec_hi, 0\n"
"L 238,36,53\n"
"s_mov_b32 exec_hi, -1\n"
"E 195,195\n"
"Svav 16,16,176,254\n"
"s_add_u32 s54, s54, 4096\n"
"Ta 90,239,41696\n"
"Ta 92,239,46304\n"
"E 196,196\n"
"E 197,197\n"
"Gvaa 168,76,216\n"
"E 198,198\n"
"Gvaa 168,72,200\n"
"buffer_load_dwordx4 a[144:147], v232, s[20:23], s54 offen sc1\n"
"E 199,199\n"
"Mvaa 208,0,60\n"
"Ta 94,239,50912\n"
"buffer_load_dwordx4 a[52:55], v250, s[20:23], s60 offen sc1\n"
"E 200,200\n"
"E 201,201\n"
"Gvaa 164,72,196\n"
"E 202,202\n"
"Gvaa 164,76,212\n"
"E 203,203\n"
"Svav 16,20,184,254\n"
"E 204,204\n"
"E 205,205\n"
"Gvaa 160,76,208\n"
"E 206,206\n"
"Gvaa 160,72,192\n"
"WL 4\n"
"E 207,207\n"
"Nzvaa 176,24,30\n"
"ds_read_b128 a[16:19], v225 offset:11600\n"
"ds_read_b128 a[20:23], v226 offset:11600\n"
"E 208,208\n"
"E 209,209\n"
"Gvaa 128,80,192\n"
"E 210,210\n"
"Gvaa 128,84,208\n"
"buffer_load_dwordx4 a[148:151], v232, s[20:23], s54 offen offset:1024 sc1\n"
"E 211,211\n"
"Svav 0,8,192,254\n"
"ds_read_b128 a[0:3], v224 offset:13920\n"
"Ta 64,239,37120\n"
"E 212,212\n"
"E 213,213\n"
"Gvaa 132,84,212\n"
"E 214,214\n"
"Gvaa 132,80,196\n"
"E 215,215\n"
"Mvaa 176,4,56\n"
"Ta 66,239,41728\n"
"Ta 68,239,46336\n"
"E 216,216\n"
"E 217,217\n"
"Gvaa 136,80,200\n"
"E 218,218\n"
"Gvaa 136,84,216\n"
"E 219,219\n"
"Svav 0,12,200,254\n"
"Ta 70,239,50944\n"
"buffer_load_dwordx4 a[152:155], v232, s[20:23], s54 offen offset:2048 sc1\n"
"E 220,220\n"
"E 221,221\n"
"Gvaa 140,84,220\n"
"E 222,222\n"
"Gvaa 140,80,204\n"
"WL 7\n"
"E 223,223\n"
"Nzvaa 192,24,34\n"
"s_add_u32 s67, s67, 4294893568\n"
"Ta 72,239,18432\n"
"Ta 74,239,23040\n"
"E 176,176\n"
"E 177,177\n"
"Gvaa 124,88,204\n"
"s_add_u32 s53, s67, 589824\n"
"E 178,178\n"
"Gvaa 124,92,220\n"
"s_add_u32 m0, s84, 73728\n"
"E 179,179\n"
"Svav 16,8,208,254\n"
"Ta 76,239,27648\n"
"Ta 78,239,32256\n"
"E 180,180\n"
"E 181,181\n"
"Gvaa 120,92,216\n"
"E 182,182\n"
"Gvaa 120,88,200\n"
"L 240,36,53\n"
"E 183,183\n"
"Mvaa 192,4,60\n"
"E 184,184\n"
"E 185,185\n"
"Gvaa 116,88,196\n"
"E 186,186\n"
"Gvaa 116,92,212\n"
"buffer_load_dwordx4 a[156:159], v232, s[20:23], s54 offen offset:3072 sc1\n"
"E 187,187\n"
"Svav 16,12,216,254\n"
"s_add_u32 s54, s64, 32768\n"
"E 188,188\n"
"E 189,189\n"
"Gvaa 112,92,208\n"
"E 190,190\n"
"Gvaa 112,88,192\n"
"WL 4\n"
"E 191,191\n"
"Nzvaa 208,26,28\n"
"s_add_u32 m0, s84, 74752\n"
"ds_read_b128 a[8:11], v225 offset:13920\n"
"ds_read_b128 a[12:15], v226 offset:13920\n"
"E 192,192\n"
"E 193,193\n"
"Gvaa 80,64,192\n"
"E 194,194\n"
"Gvaa 80,68,208\n"
"L 241,36,53\n"
"E 195,195\n"
"Svav 0,16,176,254\n"
"ds_read_b128 a[4:7], v224 offset:16240\n"
"ds_read_b32 a24, v227 offset:18560\n"
"E 196,196\n"
"E 197,197\n"
"Gvaa 84,68,212\n"
"E 198,198\n"
"Gvaa 84,64,196\n"
"E 199,199\n"
"Mvaa 208,0,56\n"
"Ta 80,239,18464\n"
"Ta 82,239,23072\n"
"E 200,200\n"
"E 201,201\n"
"Gvaa 88,64,200\n"
"E 202,202\n"
"Gvaa 88,68,216\n"
"E 203,203\n"
"Svav 0,20,184,254\n"
"Ta 84,239,27680\n"
"E 204,204\n"
"E 205,205\n"
"Gvaa 92,68,220\n"
"E 206,206\n"
"Gvaa 92,64,204\n"
"WL 7\n"
"E 207,207\n"
"Nzvaa 176,26,32\n"
"WV 14\n"
"Ta 86,239,32288\n"
"Ta 88,239,18496\n"
"E 208,208\n"
"E 209,209\n"
"Gvaa 44,72,236\n"
"s_add_u32 m0, s84, 75776\n"
"E 210,210\n"
"Gvaa 44,76,252\n"
"L 242,36,53\n"
"E 211,211\n"
"Svav 16,16,192,254\n"
"Ta 90,239,23104\n"
"Ta 92,239,27712\n"
"E 212,212\n"
"E 213,213\n"
"Gvaa 40,76,248\n"
"E 214,214\n"
"Gvaa 40,72,232\n"
"buffer_load_dwordx2 a[192:193], v232, s[20:23], s54 offen sc1\n"
"E 215,215\n"
"Mvaa 176,0,60\n"
"Ta 94,239,32320\n"
"buffer_load_dwordx2 a[194:195], v232, s[20:23], s54 offen offset:2048 sc1\n"
"E 216,216\n"
"E 217,217\n"
"Gvaa 36,72,228\n"
"E 218,218\n"
"Gvaa 36,76,244\n"
"ds_write_b128 v244, a[36:39] offset:37120\n"
"E 219,219\n"
"Svav 16,20,200,254\n"
"WV 16\n"
"ds_write_b128 v244, a[40:43] offset:38144\n"
"E 220,220\n"
"E 221,221\n"
"Gvaa 32,76,240\n"
"E 222,222\n"
"Gvaa 32,72,224\n"
"WL 6\n"
"E 223,223\n"
"Nzvaa 192,26,30\n"
"s_add_u32 m0, s84, 76800\n"
"ds_read_b128 a[16:19], v225 offset:16240\n"
"ds_read_b128 a[20:23], v226 offset:16240\n"
"E 176,176\n"
"E 177,177\n"
"Gvaa 48,80,224\n"
"E 178,178\n"
"Gvaa 48,84,240\n"
"WV 12\n"
"L 243,36,53\n"
"E 179,179\n"
"Svav 0,8,208,254\n"
"ds_read_b128 a[0:3], v224 offset:18560\n"
"Ta 64,239,18528\n"
"E 180,180\n"
"E 181,181\n"
"Gvaa 52,84,244\n"
"E 182,182\n"
"Gvaa 52,80,228\n"
"buffer_load_dwordx2 a[196:197], v232, s[20:23], s54 offen offset:8 sc1\n"
"E 183,183\n"
"Mvaa 192,4,56\n"
"Ta 66,239,23136\n"
"Ta 68,239,27744\n"
"E 184,184\n"
"E 185,185\n"
"Gvaa 56,80,232\n"
"E 186,186\n"
"Gvaa 56,84,248\n"
"buffer_load_dwordx2 a[198:199], v232, s[20:23], s54 offen offset:2056 sc1\n"
"E 187,187\n"
"Svav 0,12,216,254\n"
"Ta 70,239,32352\n"
"buffer_load_dwordx2 a[200:201], v232, s[20:23], s54 offen offset:1024 sc1\n"
"E 188,188\n"
"E 189,189\n"
"Gvaa 60,84,252\n"
"E 190,190\n"
"Gvaa 60,80,236\n"
"WL 9\n"
"buffer_load_dwordx2 a[202:203], v232, s[20:23], s54 offen offset:3072 sc1\n"
"E 191,191\n"
"Nzvaa 208,26,34\n"
"s_add_u32 m0, s84, 77824\n"
"Ta 72,239,18560\n"
"Ta 74,239,23168\n"
"E 192,192\n"
"E 193,193\n"
"Gvaa 76,88,236\n"
"E 194,194\n"
"Gvaa 76,92,252\n"
"s_mov_b32 exec_hi, 0\n"
"L 238,36,53\n"
"s_mov_b32 exec_hi, -1\n"
"E 195,195\n"
"Svav 16,8,176,254\n"
"Ta 76,239,27776\n"
"Ta 78,239,32384\n"
"E 196,196\n"
"E 197,197\n"
"Gvaa 72,92,248\n"
"E 198,198\n"
"Gvaa 72,88,232\n"
"ds_write_b128 v244, a[44:47] offset:39440\n"
"E 199,199\n"
"Mvaa 208,4,60\n"
"WV 17\n"
"ds_write_b128 v244, a[48:51] offset:40464\n"
"E 200,200\n"
"E 201,201\n"
"Gvaa 68,88,228\n"
"WV 14\n"
"E 202,202\n"
"Gvaa 68,92,244\n"
"buffer_load_dwordx2 a[204:205], v232, s[20:23], s54 offen offset:1032 sc1\n"
"E 203,203\n"
"Svav 16,12,184,254\n"
"buffer_load_dwordx2 a[206:207], v232, s[20:23], s54 offen offset:3080 sc1\n"
"ds_write_b128 v245, a[52:55] offset:37120\n"
"E 204,204\n"
"E 205,205\n"
"Gvaa 64,92,240\n"
"s_add_u32 s15, s15, 1\n"
"E 206,206\n"
"Gvaa 64,88,224\n"
"WL 7\n"
"E 207,207\n"
"Nzvaa 176,24,28\n"
"s_and_b32 s15, s15, 7\n"
"ds_read_b128 a[8:11], v225 offset:18560\n"
"ds_read_b128 a[12:15], v226 offset:18560\n"
"E 208,208\n"
"E 209,209\n"
"Gvaa 96,64,224\n"
"s_mul_i32 s60, s15, 18432\n"
"E 210,210\n"
"Gvaa 96,68,240\n"
"s_add_u32 s60, s60, s66\n"
"buffer_load_dword v229, v249, s[44:47], s73 offen\n"
"E 211,211\n"
"Svav 0,16,192,254\n"
"ds_read_b128 a[4:7], v224 offset:20880\n"
"ds_read_b32 a26, v227 offset:23200\n"
"E 212,212\n"
"E 213,213\n"
"Gvaa 100,68,244\n"
"s_add_u32 s54, s54, 4096\n"
"E 214,214\n"
"Gvaa 100,64,228\n"
"buffer_load_dwordx2 a[208:209], v232, s[20:23], s54 offen sc1\n"
"E 215,215\n"
"Mvaa 176,0,56\n"
"Ta 80,239,18592\n"
"Ta 82,239,23200\n"
"E 216,216\n"
"E 217,217\n"
"Gvaa 104,64,232\n"
"E 218,218\n"
"Gvaa 104,68,248\n"
"buffer_load_dwordx2 a[210:211], v232, s[20:23], s54 offen offset:2048 sc1\n"
"E 219,219\n"
"Svav 0,20,200,254\n"
"Ta 84,239,27808\n"
"E 220,220\n"
"E 221,221\n"
"Gvaa 108,68,252\n"
"E 222,222\n"
"Gvaa 108,64,236\n"
"WL 10\n"
"E 223,223\n"
"Nzvaa 192,24,32\n"
"Ta 86,239,32416\n"
"Ta 88,239,18624\n"
"E 176,176\n"
"E 177,177\n"
"Gvaa 156,72,236\n"
"E 178,178\n"
"Gvaa 156,76,252\n"
"buffer_load_dwordx2 a[212:213], v232, s[20:23], s54 offen offset:8 sc1\n"
"E 179,179\n"
"Svav 16,16,208,254\n"
"Ta 90,239,23232\n"
"Ta 92,239,27840\n"
"E 180,180\n"
"E 181,181\n"
"Gvaa 152,76,248\n"
"E 182,182\n"
"Gvaa 152,72,232\n"
"buffer_load_dwordx2 a[214:215], v232, s[20:23], s54 offen offset:2056 sc1\n"
"E 183,183\n"
"Mvaa 192,0,60\n"
"s_add_u32 s58, s73, 18432\n"
"Ta 94,239,32448\n"
"buffer_load_dword v229, v249, s[44:47], s58 offen\n"
"E 184,184\n"
"E 185,185\n"
"Gvaa 148,72,228\n"
"E 186,186\n"
"Gvaa 148,76,244\n"
"E 187,187\n"
"Svav 16,20,216,254\n"
"E 188,188\n"
"E 189,189\n"
"Gvaa 144,76,240\n"
"E 190,190\n"
"Gvaa 144,72,224\n"
"WL 4\n"
"E 191,191\n"
"Nzvaa 208,24,30\n"
"ds_read_b128 a[16:19], v225 offset:20880\n"
"ds_read_b128 a[20:23], v226 offset:20880\n"
"E 192,192\n"
"E 193,193\n"
"Gvaa 160,80,224\n"
"E 194,194\n"
"Gvaa 160,84,240\n"
"buffer_load_dwordx2 a[216:217], v232, s[20:23], s54 offen offset:1024 sc1\n"
"E 195,195\n"
"Svav 0,8,176,254\n"
"ds_read_b128 a[0:3], v224 offset:23200\n"
"Ta 64,239,18656\n"
"E 196,196\n"
"E 197,197\n"
"Gvaa 164,84,244\n"
"E 198,198\n"
"Gvaa 164,80,228\n"
"buffer_load_dwordx2 a[218:219], v232, s[20:23], s54 offen offset:3072 sc1\n"
"E 199,199\n"
"Mvaa 208,4,56\n"
"Ta 66,239,23264\n"
"Ta 68,239,27872\n"
"E 200,200\n"
"E 201,201\n"
"Gvaa 168,80,232\n"
"E 202,202\n"
"Gvaa 168,84,248\n"
"E 203,203\n"
"Svav 0,12,184,254\n"
"Ta 70,239,32480\n"
"buffer_load_dwordx2 a[220:221], v232, s[20:23], s54 offen offset:1032 sc1\n"
"E 204,204\n"
"E 205,205\n"
"Gvaa 172,84,252\n"
"E 206,206\n"
"Gvaa 172,80,236\n"
"WL 7\n"
"buffer_load_dwordx2 a[222:223], v232, s[20:23], s54 offen offset:3080 sc1\n"
"E 207,207\n"
"Nzvaa 176,24,34\n"
"s_add_u32 s94, s68, 73728\n"
"Ta 72,239,18688\n"
"Ta 74,239,23296\n"
"E 208,208\n"
"E 209,209\n"
"Gvaa 140,88,236\n"
"s_add_u32 s58, s73, s87\n"
"E 210,210\n"
"Gvaa 140,92,252\n"
"buffer_load_dword v229, v249, s[44:47], s58 offen\n"
"E 211,211\n"
"Svav 16,8,192,254\n"
"Ta 76,239,27904\n"
"Ta 78,239,32512\n"
"E 212,212\n"
"E 213,213\n"
"Gvaa 136,92,248\n"
"E 214,214\n"
"Gvaa 136,88,232\n"
"E 215,215\n"
"Mvaa 176,4,60\n"
"E 216,216\n"
"E 217,217\n"
"Gvaa 132,88,228\n"
"E 218,218\n"
"Gvaa 132,92,244\n"
"E 219,219\n"
"Svav 16,12,200,254\n"
"E 220,220\n"
"E 221,221\n"
"Gvaa 128,92,240\n"
"E 222,222\n"
"Gvaa 128,88,224\n"
"WL 4\n"
"E 223,223\n"
"Nzvaa 192,26,28\n"
"ds_read_b128 a[8:11], v225 offset:23200\n"
"ds_read_b128 a[12:15], v226 offset:23200\n"
"E 176,176\n"
"E 177,177\n"
"Gvaa 112,64,224\n"
"E 178,178\n"
"Gvaa 112,68,240\n"
"E 179,179\n"
"Svav 0,16,208,254\n"
"ds_read_b128 a[4:7], v224 offset:25520\n"
"ds_read_b32 a24, v227 offset:27840\n"
"E 180,180\n"
"E 181,181\n"
"Gvaa 116,68,244\n"
"E 182,182\n"
"Gvaa 116,64,228\n"
"E 183,183\n"
"Mvaa 192,0,56\n"
"E 184,184\n"
"E 185,185\n"
"Gvaa 120,64,232\n"
"E 186,186\n"
"Gvaa 120,68,248\n"
"E 187,187\n"
"Svav 0,20,216,254\n"
"E 188,188\n"
"E 189,189\n"
"Gvaa 124,68,252\n"
"E 190,190\n"
"Gvaa 124,64,236\n"
"s_waitcnt vmcnt(13) lgkmcnt(4)\n"
"E 191,191\n"
"Nzvaa 208,26,32\n"
"s_barrier\n"
"Ta 80,239,0\n"
"E 192,192\n"
"E 193,193\n"
"Gvaa 92,72,236\n"
"E 194,194\n"
"Gvaa 92,76,252\n"
"E 195,195\n"
"Svav 16,16,176,254\n"
"Ta 82,239,4608\n"
"Ta 84,239,9216\n"
"E 196,196\n"
"E 197,197\n"
"Gvaa 88,76,248\n"
"E 198,198\n"
"Gvaa 88,72,232\n"
"E 199,199\n"
"Mvaa 208,0,60\n"
"Ta 86,239,13824\n"
"Ta 88,236,0\n"
"E 200,200\n"
"E 201,201\n"
"Gvaa 84,72,228\n"
"E 202,202\n"
"Gvaa 84,76,244\n"
"E 203,203\n"
"Svav 16,20,184,254\n"
"Ta 90,236,4608\n"
"Ta 92,236,9216\n"
"E 204,204\n"
"E 205,205\n"
"Gvaa 80,76,240\n"
"E 206,206\n"
"Gvaa 80,72,224\n"
"WL 3\n"
"E 207,207\n"
"Nzvaa 176,26,30\n"
"ds_read_b128 a[16:19], v225 offset:25520\n"
"ds_read_b128 a[20:23], v226 offset:25520\n"
"E 208,208\n"
"E 209,209\n"
"Gvaa 32,80,96\n"
"s_add_u32 m0, s85, 18432\n"
"E 210,210\n"
"Gvaa 32,84,112\n"
"L 240,40,70\n"
"E 211,211\n"
"Svav 0,8,192,254\n"
"ds_read_b128 a[0:3], v224 offset:27840\n"
"Ta 94,236,13824\n"
"E 212,212\n"
"E 213,213\n"
"Gvaa 36,84,116\n"
"E 214,214\n"
"Gvaa 36,80,100\n"
"E 215,215\n"
"Mvaa 176,4,56\n"
"Ta 64,236,18432\n"
"Ta 66,236,23040\n"
"E 216,216\n"
"E 217,217\n"
"Gvaa 40,80,104\n"
"s_add_u32 m0, s85, 19456\n"
"E 218,218\n"
"Gvaa 40,84,120\n"
"L 241,40,70\n"
"E 219,219\n"
"Svav 0,12,200,254\n"
"Ta 68,236,27648\n"
"Ta 70,236,32256\n"
"E 220,220\n"
"E 221,221\n"
"Gvaa 44,84,124\n"
"E 222,222\n"
"Gvaa 44,80,108\n"
"WL 4\n"
"E 223,223\n"
"Nzvaa 192,26,34\n"
"Ta 72,239,32\n"
"Ta 74,239,4640\n"
"E 176,176\n"
"E 177,177\n"
"Gvaa 44,88,140\n"
"s_add_u32 m0, s85, 20480\n"
"E 178,178\n"
"Gvaa 44,92,156\n"
"buffer_load_dwordx4 a[36:39], v232, s[20:23], s60 offen sc1\n"
"E 179,179\n"
"Svav 16,8,208,254\n"
"Ta 76,239,9248\n"
"Ta 78,239,13856\n"
"E 180,180\n"
"E 181,181\n"
"Gvaa 40,92,152\n"
"E 182,182\n"
"Gvaa 40,88,136\n"
"L 242,40,70\n"
"E 183,183\n"
"Mvaa 192,4,60\n"
"buffer_load_dwordx4 a[40:43], v232, s[20:23], s60 offen offset:1024 sc1\n"
"E 184,184\n"
"E 185,185\n"
"Gvaa 36,88,132\n"
"s_add_u32 m0, s85, 21504\n"
"E 186,186\n"
"Gvaa 36,92,148\n"
"L 243,40,70\n"
"E 187,187\n"
"Svav 16,12,216,254\n"
"E 188,188\n"
"E 189,189\n"
"Gvaa 32,92,144\n"
"E 190,190\n"
"Gvaa 32,88,128\n"
"WL 4\n"
"E 191,191\n"
"Nzvaa 208,24,28\n"
"WV 14\n"
"ds_read_b128 a[8:11], v225 offset:27840\n"
"ds_read_b128 a[12:15], v226 offset:27840\n"
"E 192,192\n"
"E 193,193\n"
"Gvaa 32,64,192\n"
"s_add_u32 m0, s85, 22528\n"
"E 194,194\n"
"Gvaa 32,68,208\n"
"buffer_load_dwordx4 a[44:47], v232, s[20:23], s60 offen offset:2304 sc1\n"
"E 195,195\n"
"Svav 0,16,176,254\n"
"WV 13\n"
"ds_read_b128 a[4:7], v224 offset:30160\n"
"ds_read_b32 a26, v227 offset:32480\n"
"E 196,196\n"
"E 197,197\n"
"Gvaa 36,68,212\n"
"E 198,198\n"
"Gvaa 36,64,196\n"
"s_mov_b32 exec_hi, 0\n"
"L 238,40,70\n"
"s_mov_b32 exec_hi, -1\n"
"E 199,199\n"
"Mvaa 208,0,56\n"
"WV 11\n"
"Ta 80,236,32\n"
"Ta 82,236,4640\n"
"E 200,200\n"
"E 201,201\n"
"Gvaa 40,64,200\n"
"s_add_u32 s70, s70, 20736\n"
"E 202,202\n"
"Gvaa 40,68,216\n"
"s_add_u32 m0, s85, 23040\n"
"buffer_load_dwordx4 a[48:51], v232, s[20:23], s60 offen offset:3328 sc1\n"
"E 203,203\n"
"Svav 0,20,184,254\n"
"WV 10\n"
"Ta 84,236,9248\n"
"Ta 86,236,13856\n"
"E 204,204\n"
"E 205,205\n"
"Gvaa 44,68,220\n"
"E 206,206\n"
"Gvaa 44,64,204\n"
"WL 8\n"
"CV 32,32,33\n"
"E 207,207\n"
"Nzvaa 176,24,32\n"
"Ta 88,236,18464\n"
"Ta 90,236,23072\n"
"E 208,208\n"
"E 209,209\n"
"Gvaa 60,72,108\n"
"E 210,210\n"
"Gvaa 60,76,124\n"
"buffer_load_dwordx4 a[52:55], v250, s[20:23], s60 offen sc1\n"
"E 211,211\n"
"Svav 16,16,192,254\n"
"Ta 92,236,27680\n"
"Ta 94,236,32288\n"
"E 212,212\n"
"E 213,213\n"
"Gvaa 56,76,120\n"
"E 214,214\n"
"Gvaa 56,72,104\n"
"CV 33,34,35\n"
"E 215,215\n"
"Mvaa 176,0,60\n"
"CV 34,36,37\n"
"CV 35,38,39\n"
"E 216,216\n"
"E 217,217\n"
"Gvaa 52,72,100\n"
"E 218,218\n"
"Gvaa 52,76,116\n"
"buffer_store_dwordx4 v[32:35], v232, s[20:23], s68 offen sc1\n"
"E 219,219\n"
"Svav 16,20,200,254\n"
"CV 40,40,41\n"
"CV 41,42,43\n"
"E 220,220\n"
"E 221,221\n"
"Gvaa 48,76,112\n"
"E 222,222\n"
"Gvaa 48,72,96\n"
"WL 4\n"
"CV 42,44,45\n"
"E 223,223\n"
"Nzvaa 192,24,30\n"
"ds_read_b128 a[16:19], v225 offset:30160\n"
"ds_read_b128 a[20:23], v226 offset:30160\n"
"E 176,176\n"
"E 177,177\n"
"Gvaa 48,80,128\n"
"E 178,178\n"
"Gvaa 48,84,144\n"
"CV 43,46,47\n"
"E 179,179\n"
"Svav 0,8,208,254\n"
"ds_read_b128 a[0:3], v224 offset:32480\n"
"Ta 64,239,64\n"
"E 180,180\n"
"E 181,181\n"
"Gvaa 52,84,148\n"
"E 182,182\n"
"Gvaa 52,80,132\n"
"buffer_store_dwordx4 v[40:43], v232, s[20:23], s94 offen sc1\n"
"E 183,183\n"
"Mvaa 192,4,56\n"
"Ta 66,239,4672\n"
"Ta 68,239,9280\n"
"E 184,184\n"
"E 185,185\n"
"Gvaa 56,80,136\n"
"E 186,186\n"
"Gvaa 56,84,152\n"
"L 240,40,70\n"
"E 187,187\n"
"Svav 0,12,216,254\n"
"s_add_u32 m0, s85, 24064\n"
"Ta 70,239,13888\n"
"E 188,188\n"
"E 189,189\n"
"Gvaa 60,84,156\n"
"E 190,190\n"
"Gvaa 60,80,140\n"
"WL 7\n"
"L 241,40,70\n"
"E 191,191\n"
"Nzvaa 208,24,34\n"
"s_add_u32 m0, s85, 25088\n"
"Ta 72,236,64\n"
"Ta 74,236,4672\n"
"E 192,192\n"
"E 193,193\n"
"Gvaa 60,88,204\n"
"E 194,194\n"
"Gvaa 60,92,220\n"
"L 242,40,70\n"
"E 195,195\n"
"Svav 16,8,176,254\n"
"s_add_u32 m0, s85, 26112\n"
"Ta 76,236,9280\n"
"Ta 78,236,13888\n"
"E 196,196\n"
"E 197,197\n"
"Gvaa 56,92,216\n"
"E 198,198\n"
"Gvaa 56,88,200\n"
"L 243,40,70\n"
"E 199,199\n"
"Mvaa 208,4,60\n"
"s_add_u32 m0, s85, 27136\n"
"E 200,200\n"
"E 201,201\n"
"Gvaa 52,88,196\n"
"E 202,202\n"
"Gvaa 52,92,212\n"
"CV 56,56,57\n"
"E 203,203\n"
"Svav 16,12,184,254\n"
"CV 57,58,59\n"
"CV 58,60,61\n"
"E 204,204\n"
"E 205,205\n"
"Gvaa 48,92,208\n"
"E 206,206\n"
"Gvaa 48,88,192\n"
"WL 4\n"
"CV 59,62,63\n"
"E 207,207\n"
"Nzvaa 176,26,28\n"
"ds_read_b128 a[8:11], v225 offset:32480\n"
"ds_read_b128 a[12:15], v226 offset:32480\n"
"E 208,208\n"
"E 209,209\n"
"Gvaa 64,64,96\n"
"E 210,210\n"
"Gvaa 64,68,112\n"
"buffer_store_dwordx4 v[56:59], v232, s[20:23], s94 offen offset:1024 sc1\n"
"E 211,211\n"
"Svav 0,16,192,254\n"
"ds_read_b128 a[4:7], v224 offset:34800\n"
"ds_read_b32 a24, v227 offset:37120\n"
"E 212,212\n"
"E 213,213\n"
"Gvaa 68,68,116\n"
"E 214,214\n"
"Gvaa 68,64,100\n"
"CV 48,48,49\n"
"E 215,215\n"
"Mvaa 176,0,56\n"
"Ta 80,236,18496\n"
"Ta 82,236,23104\n"
"E 216,216\n"
"E 217,217\n"
"Gvaa 72,64,104\n"
"E 218,218\n"
"Gvaa 72,68,120\n"
"CV 49,50,51\n"
"E 219,219\n"
"Svav 0,20,200,254\n"
"Ta 84,236,27712\n"
"CV 50,52,53\n"
"E 220,220\n"
"E 221,221\n"
"Gvaa 76,68,124\n"
"E 222,222\n"
"Gvaa 76,64,108\n"
"WL 7\n"
"CV 51,54,55\n"
"E 223,223\n"
"Nzvaa 192,26,32\n"
"WV 14\n"
"Ta 86,236,32320\n"
"Ta 88,239,96\n"
"E 176,176\n"
"E 177,177\n"
"Gvaa 76,72,140\n"
"E 178,178\n"
"Gvaa 76,76,156\n"
"ds_write_b128 v244, a[36:39] offset:0\n"
"E 179,179\n"
"Svav 16,16,208,254\n"
"Ta 90,239,4704\n"
"Ta 92,239,9312\n"
"E 180,180\n"
"E 181,181\n"
"Gvaa 72,76,152\n"
"E 182,182\n"
"Gvaa 72,72,136\n"
"buffer_store_dwordx4 v[48:51], v232, s[20:23], s68 offen offset:1024 sc1\n"
"E 183,183\n"
"Mvaa 192,0,60\n"
"s_mov_b32 exec_hi, 0\n"
"L 238,40,70\n"
"s_mov_b32 exec_hi, -1\n"
"Ta 94,239,13920\n"
"E 184,184\n"
"E 185,185\n"
"Gvaa 68,72,132\n"
"WV 14\n"
"E 186,186\n"
"Gvaa 68,76,148\n"
"ds_write_b128 v244, a[40:43] offset:1024\n"
"E 187,187\n"
"Svav 16,20,216,254\n"
"E 188,188\n"
"E 189,189\n"
"Gvaa 64,76,144\n"
"E 190,190\n"
"Gvaa 64,72,128\n"
"WL 6\n"
"E 191,191\n"
"Nzvaa 208,26,30\n"
"ds_read_b128 a[16:19], v225 offset:34800\n"
"ds_read_b128 a[20:23], v226 offset:34800\n"
"E 192,192\n"
"E 193,193\n"
"Gvaa 64,80,192\n"
"E 194,194\n"
"Gvaa 64,84,208\n"
"WV 12\n"
"ds_write_b128 v244, a[44:47] offset:2320\n"
"E 195,195\n"
"Svav 0,8,176,254\n"
"ds_read_b128 a[0:3], v224 offset:37120\n"
"Ta 64,236,96\n"
"E 196,196\n"
"E 197,197\n"
"Gvaa 68,84,212\n"
"E 198,198\n"
"Gvaa 68,80,196\n"
"E 199,199\n"
"Mvaa 208,4,56\n"
"Ta 66,236,4704\n"
"Ta 68,236,9312\n"
"E 200,200\n"
"E 201,201\n"
"Gvaa 72,80,200\n"
"E 202,202\n"
"Gvaa 72,84,216\n"
"CV 64,64,65\n"
"E 203,203\n"
"Svav 0,12,184,254\n"
"WV 10\n"
"Ta 70,236,13920\n"
"ds_write_b128 v244, a[48:51] offset:3344\n"
"E 204,204\n"
"E 205,205\n"
"Gvaa 76,84,220\n"
"E 206,206\n"
"Gvaa 76,80,204\n"
"WL 10\n"
"CV 65,66,67\n"
"E 207,207\n"
"Nzvaa 176,26,34\n"
"Ta 72,236,18528\n"
"Ta 74,236,23136\n"
"E 208,208\n"
"E 209,209\n"
"Gvaa 108,88,108\n"
"E 210,210\n"
"Gvaa 108,92,124\n"
"CV 66,68,69\n"
"E 211,211\n"
"Svav 16,8,192,254\n"
"Ta 76,236,27744\n"
"Ta 78,236,32352\n"
"E 212,212\n"
"E 213,213\n"
"Gvaa 104,92,120\n"
"WV 9\n"
"E 214,214\n"
"Gvaa 104,88,104\n"
"ds_write_b128 v245, a[52:55] offset:0\n"
"E 215,215\n"
"Mvaa 176,4,60\n"
"s_add_u32 s15, s15, 1\n"
"CV 67,70,71\n"
"E 216,216\n"
"E 217,217\n"
"Gvaa 100,88,100\n"
"s_and_b32 s15, s15, 7\n"
"E 218,218\n"
"Gvaa 100,92,116\n"
"s_mul_i32 s60, s15, 18432\n"
"buffer_store_dwordx4 v[64:67], v232, s[20:23], s68 offen offset:2048 sc1\n"
"E 219,219\n"
"Svav 16,12,200,254\n"
"s_add_u32 s60, s60, s66\n"
"CV 72,72,73\n"
"CV 73,74,75\n"
"E 220,220\n"
"E 221,221\n"
"Gvaa 96,92,112\n"
"E 222,222\n"
"Gvaa 96,88,96\n"
"WL 6\n"
"CV 74,76,77\n"
"E 223,223\n"
"Nzvaa 192,24,28\n"
"ds_read_b128 a[8:11], v225 offset:37120\n"
"ds_read_b128 a[12:15], v226 offset:37120\n"
"E 176,176\n"
"E 177,177\n"
"Gvaa 96,64,128\n"
"E 178,178\n"
"Gvaa 96,68,144\n"
"CV 75,78,79\n"
"E 179,179\n"
"Svav 0,16,208,254\n"
"ds_read_b128 a[4:7], v224 offset:39440\n"
"ds_read_b32 a26, v227 offset:41760\n"
"E 180,180\n"
"E 181,181\n"
"Gvaa 100,68,148\n"
"E 182,182\n"
"Gvaa 100,64,132\n"
"buffer_store_dwordx4 v[72:75], v232, s[20:23], s94 offen offset:2048 sc1\n"
"E 183,183\n"
"Mvaa 192,0,56\n"
"Ta 80,239,128\n"
"Ta 82,239,4736\n"
"E 184,184\n"
"E 185,185\n"
"Gvaa 104,64,136\n"
"E 186,186\n"
"Gvaa 104,68,152\n"
"E 187,187\n"
"Svav 0,20,216,254\n"
"Ta 84,239,9344\n"
"E 188,188\n"
"E 189,189\n"
"Gvaa 108,68,156\n"
"E 190,190\n"
"Gvaa 108,64,140\n"
"WL 8\n"
"E 191,191\n"
"Nzvaa 208,24,32\n"
"Ta 86,239,13952\n"
"Ta 88,236,128\n"
"E 192,192\n"
"E 193,193\n"
"Gvaa 108,72,204\n"
"E 194,194\n"
"Gvaa 108,76,220\n"
"E 195,195\n"
"Svav 16,16,176,254\n"
"Ta 90,236,4736\n"
"Ta 92,236,9344\n"
"E 196,196\n"
"E 197,197\n"
"Gvaa 104,76,216\n"
"E 198,198\n"
"Gvaa 104,72,200\n"
"E 199,199\n"
"Mvaa 208,0,60\n"
"WV 2\n"
"Ta 94,236,13952\n"
"Ta 224,237,18432\n"
"E 200,200\n"
"E 201,201\n"
"Gvaa 100,72,196\n"
"E 202,202\n"
"Gvaa 100,76,212\n"
"CV 104,104,105\n"
"E 203,203\n"
"Svav 16,20,184,254\n"
"CV 105,106,107\n"
"CV 106,108,109\n"
"E 204,204\n"
"E 205,205\n"
"Gvaa 96,76,208\n"
"E 206,206\n"
"Gvaa 96,72,192\n"
"WL 5\n"
"CV 107,110,111\n"
"E 207,207\n"
"Nzvaa 176,24,30\n"
"ds_read_b128 a[16:19], v225 offset:39440\n"
"ds_read_b128 a[20:23], v226 offset:39440\n"
"E 208,208\n"
"E 209,209\n"
"Gvaa 144,80,96\n"
"E 210,210\n"
"Gvaa 144,84,112\n"
"buffer_store_dwordx4 v[104:107], v232, s[20:23], s94 offen offset:3072 sc1\n"
"E 211,211\n"
"Svav 0,8,192,254\n"
"ds_read_b128 a[0:3], v224 offset:41760\n"
"Ta 64,236,18560\n"
"E 212,212\n"
"E 213,213\n"
"Gvaa 148,84,116\n"
"E 214,214\n"
"Gvaa 148,80,100\n"
"CV 96,96,97\n"
"E 215,215\n"
"Mvaa 176,4,56\n"
"Ta 66,236,23168\n"
"Ta 68,236,27776\n"
"E 216,216\n"
"E 217,217\n"
"Gvaa 152,80,104\n"
"E 218,218\n"
"Gvaa 152,84,120\n"
"CV 97,98,99\n"
"E 219,219\n"
"Svav 0,12,200,254\n"
"Ta 70,236,32384\n"
"CV 98,100,101\n"
"E 220,220\n"
"E 221,221\n"
"Gvaa 156,84,124\n"
"E 222,222\n"
"Gvaa 156,80,108\n"
"WL 8\n"
"CV 99,102,103\n"
"E 223,223\n"
"Nzvaa 192,24,34\n"
"s_barrier\n"
"Ta 72,239,160\n"
"E 176,176\n"
"E 177,177\n"
"Gvaa 156,88,140\n"
"E 178,178\n"
"Gvaa 156,92,156\n"
"buffer_store_dwordx4 v[96:99], v232, s[20:23], s68 offen offset:3072 sc1\n"
"E 179,179\n"
"Svav 16,8,208,254\n"
"s_add_u32 s68, s68, 4096\n"
"Ta 74,239,4768\n"
"Ta 76,239,9376\n"
"E 180,180\n"
"E 181,181\n"
"Gvaa 152,92,152\n"
"E 182,182\n"
"Gvaa 152,88,136\n"
"Ta 226,237,20736\n"
"E 183,183\n"
"Mvaa 192,4,60\n"
"Ta 78,239,13984\n"
"Ta 228,237,18464\n"
"E 184,184\n"
"E 185,185\n"
"Gvaa 148,88,132\n"
"E 186,186\n"
"Gvaa 148,92,148\n"
"Ta 230,237,20768\n"
"E 187,187\n"
"Svav 16,12,216,254\n"
"Ta 232,237,18496\n"
"Ta 234,237,20800\n"
"E 188,188\n"
"E 189,189\n"
"Gvaa 144,92,144\n"
"E 190,190\n"
"Gvaa 144,88,128\n"
"WL 9\n"
"Ta 236,237,18528\n"
"E 191,191\n"
"Nzvaa 208,26,28\n"
"ds_read_b128 a[8:11], v225 offset:41760\n"
"ds_read_b128 a[12:15], v226 offset:41760\n"
"E 192,192\n"
"E 193,193\n"
"Gvaa 144,64,192\n"
"E 194,194\n"
"Gvaa 144,68,208\n"
"Ta 238,237,20832\n"
"E 195,195\n"
"Svav 0,16,176,254\n"
"ds_read_b128 a[4:7], v224 offset:44080\n"
"ds_read_b32 a24, v227 offset:46400\n"
"E 196,196\n"
"E 197,197\n"
"Gvaa 148,68,212\n"
"E 198,198\n"
"Gvaa 148,64,196\n"
"Ta 240,237,18560\n"
"E 199,199\n"
"Mvaa 208,0,56\n"
"Ta 80,236,160\n"
"Ta 82,236,4768\n"
"E 200,200\n"
"E 201,201\n"
"Gvaa 152,64,200\n"
"E 202,202\n"
"Gvaa 152,68,216\n"
"CV 144,144,145\n"
"E 203,203\n"
"Svav 0,20,184,254\n"
"Ta 84,236,9376\n"
"CV 145,146,147\n"
"E 204,204\n"
"E 205,205\n"
"Gvaa 156,68,220\n"
"E 206,206\n"
"Gvaa 156,64,204\n"
"WL 14\n"
"CV 146,148,149\n"
"E 207,207\n"
"Nzvaa 176,26,32\n"
"Ta 86,236,13984\n"
"Ta 88,236,18592\n"
"E 208,208\n"
"E 209,209\n"
"Gvaa 172,72,108\n"
"E 210,210\n"
"Gvaa 172,76,124\n"
"buffer_load_dwordx4 a[36:39], v232, s[20:23], s60 offen sc1\n"
"E 211,211\n"
"Svav 16,16,192,254\n"
"Ta 90,236,23200\n"
"Ta 92,236,27808\n"
"E 212,212\n"
"E 213,213\n"
"Gvaa 168,76,120\n"
"E 214,214\n"
"Gvaa 168,72,104\n"
"CV 147,150,151\n"
"E 215,215\n"
"Mvaa 176,0,60\n"
"Ta 94,236,32416\n"
"buffer_load_dwordx4 a[40:43], v232, s[20:23], s60 offen offset:1024 sc1\n"
"E 216,216\n"
"E 217,217\n"
"Gvaa 164,72,100\n"
"E 218,218\n"
"Gvaa 164,76,116\n"
"buffer_store_dwordx4 v[144:147], v232, s[20:23], s68 offen sc1\n"
"E 219,219\n"
"Svav 16,20,200,254\n"
"s_add_u32 s94, s94, 4096\n"
"CV 152,152,153\n"
"CV 153,154,155\n"
"E 220,220\n"
"E 221,221\n"
"Gvaa 160,76,112\n"
"E 222,222\n"
"Gvaa 160,72,96\n"
"WL 4\n"
"CV 154,156,157\n"
"E 223,223\n"
"Nzvaa 192,26,30\n"
"ds_read_b128 a[16:19], v225 offset:44080\n"
"ds_read_b128 a[20:23], v226 offset:44080\n"
"E 176,176\n"
"E 177,177\n"
"Gvaa 160,80,128\n"
"E 178,178\n"
"Gvaa 160,84,144\n"
"buffer_load_dwordx4 a[44:47], v232, s[20:23], s60 offen offset:2304 sc1\n"
"E 179,179\n"
"Svav 0,8,208,254\n"
"ds_read_b128 a[0:3], v224 offset:46400\n"
"Ta 64,239,192\n"
"E 180,180\n"
"E 181,181\n"
"Gvaa 164,84,148\n"
"E 182,182\n"
"Gvaa 164,80,132\n"
"CV 155,158,159\n"
"E 183,183\n"
"Mvaa 192,4,56\n"
"Ta 66,239,4800\n"
"Ta 68,239,9408\n"
"E 184,184\n"
"E 185,185\n"
"Gvaa 168,80,136\n"
"E 186,186\n"
"Gvaa 168,84,152\n"
"buffer_load_dwordx4 a[48:51], v232, s[20:23], s60 offen offset:3328 sc1\n"
"E 187,187\n"
"Svav 0,12,216,254\n"
"Ta 70,239,14016\n"
"buffer_store_dwordx4 v[152:155], v232, s[20:23], s94 offen sc1\n"
"E 188,188\n"
"E 189,189\n"
"Gvaa 172,84,156\n"
"E 190,190\n"
"Gvaa 172,80,140\n"
"WL 7\n"
"Ta 242,237,20864\n"
"E 191,191\n"
"Nzvaa 208,26,34\n"
"Ta 72,236,192\n"
"Ta 74,236,4800\n"
"E 192,192\n"
"E 193,193\n"
"Gvaa 172,88,204\n"
"E 194,194\n"
"Gvaa 172,92,220\n"
"buffer_load_dwordx4 a[52:55], v250, s[20:23], s60 offen sc1\n"
"E 195,195\n"
"Svav 16,8,176,254\n"
"Ta 76,236,9408\n"
"Ta 78,236,14016\n"
"E 196,196\n"
"E 197,197\n"
"Gvaa 168,92,216\n"
"E 198,198\n"
"Gvaa 168,88,200\n"
"Ta 244,237,18592\n"
"E 199,199\n"
"Mvaa 208,4,60\n"
"Ta 246,237,20896\n"
"Ta 248,237,18624\n"
"E 200,200\n"
"E 201,201\n"
"Gvaa 164,88,196\n"
"E 202,202\n"
"Gvaa 164,92,212\n"
"CV 168,168,169\n"
"E 203,203\n"
"Svav 16,12,184,254\n"
"CV 169,170,171\n"
"CV 170,172,173\n"
"E 204,204\n"
"E 205,205\n"
"Gvaa 160,92,208\n"
"E 206,206\n"
"Gvaa 160,88,192\n"
"WL 8\n"
"CV 171,174,175\n"
"E 207,207\n"
"Nzvaa 176,24,28\n"
"ds_read_b128 a[8:11], v225 offset:46400\n"
"ds_read_b128 a[12:15], v226 offset:46400\n"
"E 208,208\n"
"E 209,209\n"
"Gvaa 128,64,96\n"
"E 210,210\n"
"Gvaa 128,68,112\n"
"buffer_store_dwordx4 v[168:171], v232, s[20:23], s94 offen offset:1024 sc1\n"
"E 211,211\n"
"Svav 0,16,192,254\n"
"ds_read_b128 a[4:7], v224 offset:48720\n"
"ds_read_b32 a26, v227 offset:51040\n"
"E 212,212\n"
"E 213,213\n"
"Gvaa 132,68,116\n"
"E 214,214\n"
"Gvaa 132,64,100\n"
"CV 160,160,161\n"
"E 215,215\n"
"Mvaa 176,0,56\n"
"Ta 80,236,18624\n"
"Ta 82,236,23232\n"
"E 216,216\n"
"E 217,217\n"
"Gvaa 136,64,104\n"
"E 218,218\n"
"Gvaa 136,68,120\n"
"CV 161,162,163\n"
"E 219,219\n"
"Svav 0,20,200,254\n"
"Ta 84,236,27840\n"
"CV 162,164,165\n"
"E 220,220\n"
"E 221,221\n"
"Gvaa 140,68,124\n"
"E 222,222\n"
"Gvaa 140,64,108\n"
"WL 10\n"
"CV 163,166,167\n"
"E 223,223\n"
"Nzvaa 192,24,32\n"
"Ta 86,236,32448\n"
"Ta 88,239,224\n"
"E 176,176\n"
"E 177,177\n"
"Gvaa 140,72,140\n"
"E 178,178\n"
"Gvaa 140,76,156\n"
"buffer_store_dwordx4 v[160:163], v232, s[20:23], s68 offen offset:1024 sc1\n"
"E 179,179\n"
"Svav 16,16,208,254\n"
"Ta 90,239,4832\n"
"Ta 92,239,9440\n"
"E 180,180\n"
"E 181,181\n"
"Gvaa 136,76,152\n"
"E 182,182\n"
"Gvaa 136,72,136\n"
"ds_read_b128 v[160:163], v234 offset:7168\n"
"E 183,183\n"
"Mvaa 192,0,60\n"
"Ta 94,239,14048\n"
"ds_read_b96 v[170:172], v234 offset:9216\n"
"E 184,184\n"
"E 185,185\n"
"Gvaa 132,72,132\n"
"E 186,186\n"
"Gvaa 132,76,148\n"
"Ta 250,237,20928\n"
"E 187,187\n"
"Svav 16,20,216,254\n"
"Ta 252,237,18656\n"
"Ta 254,237,20960\n"
"E 188,188\n"
"E 189,189\n"
"Gvaa 128,76,144\n"
"E 190,190\n"
"Gvaa 128,72,128\n"
"WL 9\n"
"Ta 160,237,18688\n"
"E 191,191\n"
"Nzvaa 208,24,30\n"
"ds_read_b128 a[16:19], v225 offset:48720\n"
"ds_read_b128 a[20:23], v226 offset:48720\n"
"E 192,192\n"
"E 193,193\n"
"Gvaa 128,80,192\n"
"E 194,194\n"
"Gvaa 128,84,208\n"
"Ta 162,237,20992\n"
"E 195,195\n"
"Svav 0,8,176,254\n"
"ds_read_b128 a[0:3], v224 offset:51040\n"
"Ta 64,236,224\n"
"E 196,196\n"
"E 197,197\n"
"Gvaa 132,84,212\n"
"E 198,198\n"
"Gvaa 132,80,196\n"
"E 199,199\n"
"Mvaa 208,4,56\n"
"WL 9\n"
"Ta 66,236,4832\n"
"Ta 68,236,9440\n"
"E 200,200\n"
"E 201,201\n"
"Gvaa 136,80,200\n"
"E 202,202\n"
"Gvaa 136,84,216\n"
"WL 4\n"
"CV 128,128,129\n"
"E 203,203\n"
"Svav 0,12,184,254\n"
"s_add_u32 s70, s70, 20736\n"
"Ta 70,236,14048\n"
"CV 129,130,131\n"
"E 204,204\n"
"E 205,205\n"
"Gvaa 140,84,220\n"
"s_add_u32 m0, s85, 18432\n"
"E 206,206\n"
"Gvaa 140,80,204\n"
"CV 130,132,133\n"
"E 207,207\n"
"Nzvaa 176,24,34\n"
"WV 8\n"
"Ta 72,236,18656\n"
"Ta 74,236,23264\n"
"E 208,208\n"
"E 209,209\n"
"Gvaa 124,88,108\n"
"E 210,210\n"
"Gvaa 124,92,124\n"
"ds_write_b128 v244, a[36:39] offset:18560\n"
"E 211,211\n"
"Svav 16,8,192,254\n"
"Ta 76,236,27872\n"
"Ta 78,236,32480\n"
"E 212,212\n"
"E 213,213\n"
"Gvaa 120,92,120\n"
"E 214,214\n"
"Gvaa 120,88,104\n"
"CV 131,134,135\n"
"E 215,215\n"
"Mvaa 176,4,60\n"
"buffer_store_dwordx4 v[128:131], v232, s[20:23], s68 offen offset:2048 sc1\n"
"CV 136,136,137\n"
"E 216,216\n"
"E 217,217\n"
"Gvaa 116,88,100\n"
"WV 8\n"
"E 218,218\n"
"Gvaa 116,92,116\n"
"ds_write_b128 v244, a[40:43] offset:19584\n"
"E 219,219\n"
"Svav 16,12,200,254\n"
"CV 137,138,139\n"
"CV 138,140,141\n"
"E 220,220\n"
"E 221,221\n"
"Gvaa 112,92,112\n"
"E 222,222\n"
"Gvaa 112,88,96\n"
"WL 6\n"
"CV 139,142,143\n"
"E 223,223\n"
"Nzvaa 192,26,28\n"
"ds_read_b128 a[8:11], v225 offset:51040\n"
"ds_read_b128 a[12:15], v226 offset:51040\n"
"E 176,176\n"
"E 177,177\n"
"Gvaa 112,64,128\n"
"E 178,178\n"
"Gvaa 112,68,144\n"
"WV 6\n"
"ds_write_b128 v244, a[44:47] offset:20880\n"
"E 179,179\n"
"Svav 0,16,208,254\n"
"ds_read_b128 a[4:7], v224 offset:53360\n"
"ds_read_b32 a24, v227 offset:0\n"
"E 180,180\n"
"E 181,181\n"
"Gvaa 116,68,148\n"
"E 182,182\n"
"Gvaa 116,64,132\n"
"buffer_store_dwordx4 v[136:139], v232, s[20:23], s94 offen offset:2048 sc1\n"
"E 183,183\n"
"Mvaa 192,0,56\n"
"Ta 80,239,256\n"
"Ta 82,239,4864\n"
"E 184,184\n"
"E 185,185\n"
"Gvaa 120,64,136\n"
"E 186,186\n"
"Gvaa 120,68,152\n"
"L 240,40,70\n"
"E 187,187\n"
"Svav 0,20,216,254\n"
"WV 7\n"
"Ta 84,239,9472\n"
"ds_write_b128 v244, a[48:51] offset:21904\n"
"E 188,188\n"
"E 189,189\n"
"Gvaa 124,68,156\n"
"s_add_u32 m0, s85, 19456\n"
"E 190,190\n"
"Gvaa 124,64,140\n"
"WL 10\n"
"L 241,40,70\n"
"E 191,191\n"
"Nzvaa 208,26,32\n"
"s_add_u32 m0, s85, 20480\n"
"Ta 86,239,14080\n"
"Ta 88,236,256\n"
"E 192,192\n"
"E 193,193\n"
"Gvaa 124,72,204\n"
"E 194,194\n"
"Gvaa 124,76,220\n"
"L 242,40,70\n"
"E 195,195\n"
"Svav 16,16,176,254\n"
"s_add_u32 m0, s85, 21504\n"
"Ta 90,236,4864\n"
"Ta 92,236,9472\n"
"E 196,196\n"
"E 197,197\n"
"Gvaa 120,76,216\n"
"WV 7\n"
"E 198,198\n"
"Gvaa 120,72,200\n"
"ds_write_b128 v245, a[52:55] offset:18560\n"
"E 199,199\n"
"Mvaa 208,0,60\n"
"s_add_u32 s15, s15, 1\n"
"Ta 94,236,14080\n"
"L 243,40,70\n"
"E 200,200\n"
"E 201,201\n"
"Gvaa 116,72,196\n"
"s_and_b32 s15, s15, 7\n"
"E 202,202\n"
"Gvaa 116,76,212\n"
"s_mul_i32 s60, s15, 18432\n"
"CV 120,120,121\n"
"E 203,203\n"
"Svav 16,20,184,254\n"
"s_add_u32 s60, s60, s66\n"
"CV 121,122,123\n"
"CV 122,124,125\n"
"E 204,204\n"
"E 205,205\n"
"Gvaa 112,76,208\n"
"s_add_u32 m0, s85, 22528\n"
"E 206,206\n"
"Gvaa 112,72,192\n"
"WL 5\n"
"CV 123,126,127\n"
"E 207,207\n"
"Nzvaa 176,26,30\n"
"ds_read_b128 a[16:19], v225 offset:53360\n"
"ds_read_b128 a[20:23], v226 offset:53360\n"
"E 208,208\n"
"E 209,209\n"
"Gvaa 80,80,96\n"
"E 210,210\n"
"Gvaa 80,84,112\n"
"buffer_store_dwordx4 v[120:123], v232, s[20:23], s94 offen offset:3072 sc1\n"
"E 211,211\n"
"Svav 0,8,192,254\n"
"ds_read_b128 a[0:3], v224 offset:0\n"
"Ta 64,236,18688\n"
"E 212,212\n"
"E 213,213\n"
"Gvaa 84,84,116\n"
"E 214,214\n"
"Gvaa 84,80,100\n"
"CV 112,112,113\n"
"E 215,215\n"
"Mvaa 176,4,56\n"
"Ta 66,236,23296\n"
"Ta 68,236,27904\n"
"E 216,216\n"
"E 217,217\n"
"Gvaa 88,80,104\n"
"E 218,218\n"
"Gvaa 88,84,120\n"
"CV 113,114,115\n"
"E 219,219\n"
"Svav 0,12,200,254\n"
"Ta 70,236,32512\n"
"CV 114,116,117\n"
"E 220,220\n"
"E 221,221\n"
"Gvaa 92,84,124\n"
"E 222,222\n"
"Gvaa 92,80,108\n"
"WL 7\n"
"CV 115,118,119\n"
"E 223,223\n"
"Nzvaa 192,26,34\n"
"s_mov_b32 exec_hi, 0\n"
"L 238,40,70\n"
"s_mov_b32 exec_hi, -1\n"
"buffer_store_dwordx4 v[112:115], v232, s[20:23], s68 offen offset:3072 sc1\n"
"E 176,176\n"
"E 177,177\n"
"Gvaa 92,88,140\n"
"s_add_u32 s68, s68, 4096\n"
"E 178,178\n"
"Gvaa 92,92,156\n"
"s_add_u32 s70, s70, 20736\n"
"E 179,179\n"
"Svav 16,8,208,254\n"
"s_add_u32 m0, s85, 23040\n"
"E 180,180\n"
"E 181,181\n"
"Gvaa 88,92,152\n"
"E 182,182\n"
"Gvaa 88,88,136\n"
"L 240,40,70\n"
"E 183,183\n"
"Mvaa 192,4,60\n"
"s_add_u32 m0, s85, 24064\n"
"E 184,184\n"
"E 185,185\n"
"Gvaa 84,88,132\n"
"E 186,186\n"
"Gvaa 84,92,148\n"
"L 241,40,70\n"
"E 187,187\n"
"Svav 16,12,216,254\n"
"s_add_u32 m0, s85, 25088\n"
"E 188,188\n"
"E 189,189\n"
"Gvaa 80,92,144\n"
"E 190,190\n"
"Gvaa 80,88,128\n"
"WL 0\n"
"L 242,40,70\n"
"E 191,191\n"
"Nzvaa 208,24,28\n"
"s_add_u32 m0, s85, 26112\n"
"ds_read_b128 a[8:11], v225 offset:0\n"
"ds_read_b128 a[12:15], v226 offset:0\n"
"E 192,192\n"
"E 193,193\n"
"Gvaa 80,64,192\n"
"E 194,194\n"
"Gvaa 80,68,208\n"
"L 243,40,70\n"
"E 195,195\n"
"Svav 0,16,176,254\n"
"s_add_u32 m0, s85, 27136\n"
"ds_read_b128 a[4:7], v224 offset:2320\n"
"ds_read_b32 a26, v227 offset:4640\n"
"E 196,196\n"
"E 197,197\n"
"Gvaa 84,68,212\n"
"E 198,198\n"
"Gvaa 84,64,196\n"
"s_mov_b32 exec_hi, 0\n"
"L 238,40,70\n"
"s_mov_b32 exec_hi, -1\n"
"E 199,199\n"
"Mvaa 208,0,56\n"
"E 200,200\n"
"E 201,201\n"
"Gvaa 88,64,200\n"
"E 202,202\n"
"Gvaa 88,68,216\n"
"CV 80,80,81\n"
"E 203,203\n"
"Svav 0,20,184,254\n"
"CV 81,82,83\n"
"CV 82,84,85\n"
"E 204,204\n"
"E 205,205\n"
"Gvaa 92,68,220\n"
"E 206,206\n"
"Gvaa 92,64,204\n"
"CV 83,86,87\n"
"E 207,207\n"
"Nzvaa 176,24,32\n"
"s_barrier\n"
"buffer_store_dwordx4 v[80:83], v232, s[20:23], s68 offen sc1\n"
"E 208,208\n"
"E 209,209\n"
"E 210,210\n"
"E 211,211\n"
"Svav 16,16,192,254\n"
"s_add_u32 s94, s94, 4096\n"
"v_add_u32 v239, s84, v239\n"
"E 212,212\n"
"E 213,213\n"
"E 214,214\n"
"E 215,215\n"
"Mvaa 176,0,60\n"
"s_add_u32 s70, s70, 20736\n"
"CV 88,88,89\n"
"CV 89,90,91\n"
"E 216,216\n"
"E 217,217\n"
"E 218,218\n"
"E 219,219\n"
"Svav 16,20,200,254\n"
"WL 1\n"
"CV 90,92,93\n"
"CV 91,94,95\n"
"E 220,220\n"
"E 221,221\n"
"E 222,222\n"
"E 223,223\n"
"Nzvaa 192,24,30\n"
"s_add_u32 m0, s85, 55296\n"
"ds_read_b128 a[16:19], v225 offset:2320\n"
"buffer_store_dwordx4 v[88:91], v232, s[20:23], s94 offen sc1\n"
"E 176,176\n"
"E 177,177\n"
"E 178,178\n"
"E 179,179\n"
"Svav 0,8,208,254\n"
"ds_read_b128 a[20:23], v226 offset:2320\n"
"L 240,40,70\n"
"E 180,180\n"
"E 181,181\n"
"E 182,182\n"
"E 183,183\n"
"Mvaa 192,4,56\n"
"s_add_u32 m0, s85, 56320\n"
"ds_read_b128 a[0:3], v224 offset:4640\n"
"E 184,184\n"
"E 185,185\n"
"E 186,186\n"
"E 187,187\n"
"Svav 0,12,216,254\n"
"L 241,40,70\n"
"E 188,188\n"
"E 189,189\n"
"E 190,190\n"
"E 191,191\n"
"Nzvaa 208,24,34\n"
"s_add_u32 m0, s85, 57344\n"
"E 192,192\n"
"E 193,193\n"
"E 194,194\n"
"E 195,195\n"
"Svav 16,8,176,254\n"
"L 242,40,70\n"
"E 196,196\n"
"E 197,197\n"
"E 198,198\n"
"E 199,199\n"
"Mvaa 208,4,60\n"
"s_add_u32 m0, s85, 58368\n"
"E 200,200\n"
"E 201,201\n"
"E 202,202\n"
"E 203,203\n"
"Svav 16,12,184,254\n"
"WL 0\n"
"L 243,40,70\n"
"E 204,204\n"
"E 205,205\n"
"E 206,206\n"
"E 207,207\n"
"Nzvaa 176,26,28\n"
"s_add_u32 m0, s85, 59392\n"
"ds_read_b128 a[8:11], v225 offset:4640\n"
"E 208,208\n"
"E 209,209\n"
"E 210,210\n"
"E 211,211\n"
"Svav 0,16,192,254\n"
"s_mov_b32 exec_hi, 0\n"
"L 238,40,70\n"
"s_mov_b32 exec_hi, -1\n"
"ds_read_b128 a[12:15], v226 offset:4640\n"
"E 212,212\n"
"E 213,213\n"
"E 214,214\n"
"E 215,215\n"
"Mvaa 176,0,56\n"
"WV 7\n"
"ds_read_b128 a[4:7], v224 offset:6960\n"
"Tv 32,237,18432\n"
"E 216,216\n"
"E 217,217\n"
"E 218,218\n"
"E 219,219\n"
"Svav 0,20,200,254\n"
"s_add_u32 s70, s70, 20736\n"
"ds_read_b32 a24, v227 offset:9280\n"
"Tv 34,237,20736\n"
"E 220,220\n"
"E 221,221\n"
"E 222,222\n"
"E 223,223\n"
"Nzvaa 192,26,32\n"
"s_add_u32 m0, s85, 59904\n"
"buffer_load_dwordx4 a[36:39], v232, s[20:23], s60 offen sc1\n"
"Tv 36,237,18464\n"
"E 176,176\n"
"E 177,177\n"
"E 178,178\n"
"E 179,179\n"
"Svav 16,16,208,254\n"
"Tv 38,237,20768\n"
"Tv 40,237,18496\n"
"E 180,180\n"
"E 181,181\n"
"E 182,182\n"
"E 183,183\n"
"Mvaa 192,0,60\n"
"buffer_load_dwordx4 a[40:43], v232, s[20:23], s60 offen offset:1024 sc1\n"
"Tv 42,237,20800\n"
"E 184,184\n"
"E 185,185\n"
"E 186,186\n"
"E 187,187\n"
"Svav 16,20,216,254\n"
"WL 7\n"
"Tv 44,237,18528\n"
"Tv 46,237,20832\n"
"E 188,188\n"
"E 189,189\n"
"E 190,190\n"
"E 191,191\n"
"Nzvaa 208,26,30\n"
"ds_read_b128 a[16:19], v225 offset:6960\n"
"buffer_load_dwordx4 a[44:47], v232, s[20:23], s60 offen offset:2304 sc1\n"
"E 192,192\n"
"E 193,193\n"
"E 194,194\n"
"E 195,195\n"
"Svav 0,8,176,254\n"
"ds_read_b128 a[20:23], v226 offset:6960\n"
"Tv 48,237,18560\n"
"E 196,196\n"
"E 197,197\n"
"E 198,198\n"
"E 199,199\n"
"Mvaa 208,4,56\n"
"ds_read_b128 a[0:3], v224 offset:9280\n"
"buffer_load_dwordx4 a[48:51], v232, s[20:23], s60 offen offset:3328 sc1\n"
"E 200,200\n"
"E 201,201\n"
"E 202,202\n"
"E 203,203\n"
"Svav 0,12,184,254\n"
"Tv 50,237,20864\n"
"Tv 52,237,18592\n"
"E 204,204\n"
"E 205,205\n"
"E 206,206\n"
"E 207,207\n"
"Nzvaa 176,26,34\n"
"buffer_load_dwordx4 a[52:55], v250, s[20:23], s60 offen sc1\n"
"Tv 54,237,20896\n"
"E 208,208\n"
"E 209,209\n"
"E 210,210\n"
"E 211,211\n"
"Svav 16,8,192,254\n"
"Tv 56,237,18624\n"
"Tv 58,237,20928\n"
"E 212,212\n"
"E 213,213\n"
"E 214,214\n"
"E 215,215\n"
"Mvaa 176,4,60\n"
"WV 10\n"
"buffer_store_dwordx2 v[230:231], v252, s[20:23], s74 offen offset:32 sc1\n"
"Tv 60,237,18656\n"
"E 216,216\n"
"E 217,217\n"
"E 218,218\n"
"E 219,219\n"
"Svav 16,12,200,254\n"
"WL 6\n"
"Tv 62,237,20960\n"
"Tv 64,237,18688\n"
"E 220,220\n"
"E 221,221\n"
"E 222,222\n"
"E 223,223\n"
"Nzvaa 192,24,28\n"
"ds_read_b128 a[8:11], v225 offset:9280\n"
"Tv 66,237,20992\n"
"E 176,176\n"
"E 177,177\n"
"E 178,178\n"
"E 179,179\n"
"Svav 0,16,208,254\n"
"ds_read_b128 a[12:15], v226 offset:9280\n"
"L 240,40,70\n"
"E 180,180\n"
"E 181,181\n"
"E 182,182\n"
"E 183,183\n"
"Mvaa 192,0,56\n"
"s_add_u32 m0, s85, 60928\n"
"ds_read_b128 a[4:7], v224 offset:11600\n"
"E 184,184\n"
"E 185,185\n"
"E 186,186\n"
"E 187,187\n"
"Svav 0,20,216,254\n"
"ds_read_b32 a26, v227 offset:13920\n"
"L 241,40,70\n"
"E 188,188\n"
"E 189,189\n"
"E 190,190\n"
"E 191,191\n"
"Nzvaa 208,24,32\n"
"s_add_u32 m0, s85, 61952\n"
"E 192,192\n"
"E 193,193\n"
"E 194,194\n"
"E 195,195\n"
"Svav 16,16,176,254\n"
"L 242,40,70\n"
"E 196,196\n"
"E 197,197\n"
"E 198,198\n"
"E 199,199\n"
"Mvaa 208,0,60\n"
"s_add_u32 m0, s85, 62976\n"
"E 200,200\n"
"E 201,201\n"
"E 202,202\n"
"E 203,203\n"
"Svav 16,20,184,254\n"
"WL 1\n"
"L 243,40,70\n"
"E 204,204\n"
"E 205,205\n"
"E 206,206\n"
"E 207,207\n"
"Nzvaa 176,24,30\n"
"s_add_u32 m0, s85, 64000\n"
"ds_read_b128 a[16:19], v225 offset:11600\n"
"E 208,208\n"
"E 209,209\n"
"E 210,210\n"
"E 211,211\n"
"Svav 0,8,192,254\n"
"s_mov_b32 exec_hi, 0\n"
"L 238,40,70\n"
"s_mov_b32 exec_hi, -1\n"
"ds_read_b128 a[20:23], v226 offset:11600\n"
"E 212,212\n"
"E 213,213\n"
"E 214,214\n"
"E 215,215\n"
"Mvaa 176,4,56\n"
"ds_read_b128 a[0:3], v224 offset:13920\n"
"E 216,216\n"
"E 217,217\n"
"E 218,218\n"
"E 219,219\n"
"Svav 0,12,200,254\n"
"E 220,220\n"
"E 221,221\n"
"E 222,222\n"
"E 223,223\n"
"Nzvaa 192,24,34\n"
"WV 10\n"
"ds_write_b128 v244, a[36:39] offset:37120\n"
"E 176,176\n"
"E 177,177\n"
"E 178,178\n"
"E 179,179\n"
"Svav 16,8,208,254\n"
"s_add_u32 s70, s70, 4294863616\n"
"E 180,180\n"
"E 181,181\n"
"E 182,182\n"
"E 183,183\n"
"Mvaa 192,4,60\n"
"WV 9\n"
"ds_write_b128 v244, a[40:43] offset:38144\n"
"E 184,184\n"
"E 185,185\n"
"E 186,186\n"
"E 187,187\n"
"Svav 16,12,216,254\n"
"WL 2\n"
"E 188,188\n"
"E 189,189\n"
"E 190,190\n"
"E 191,191\n"
"Nzvaa 208,26,28\n"
"WV 8\n"
"ds_read_b128 a[8:11], v225 offset:13920\n"
"ds_write_b128 v244, a[44:47] offset:39440\n"
"E 192,192\n"
"E 193,193\n"
"E 194,194\n"
"E 195,195\n"
"Svav 0,16,176,254\n"
"s_add_u32 m0, s84, 0\n"
"ds_read_b128 a[12:15], v226 offset:13920\n"
"E 196,196\n"
"E 197,197\n"
"E 198,198\n"
"E 199,199\n"
"Mvaa 208,0,56\n"
"ds_read_b128 a[4:7], v224 offset:16240\n"
"L 160,40,70\n"
"E 200,200\n"
"E 201,201\n"
"E 202,202\n"
"E 203,203\n"
"Svav 0,20,184,254\n"
"WV 8\n"
"ds_read_b32 a24, v227 offset:18560\n"
"ds_write_b128 v244, a[48:51] offset:40464\n"
"E 204,204\n"
"E 205,205\n"
"E 206,206\n"
"E 207,207\n"
"Nzvaa 176,26,32\n"
"s_add_u32 m0, s84, 1024\n"
"E 208,208\n"
"E 209,209\n"
"E 210,210\n"
"E 211,211\n"
"Svav 16,16,192,254\n"
"WV 7\n"
"ds_write_b128 v245, a[52:55] offset:37120\n"
"L 161,40,70\n"
"E 212,212\n"
"E 213,213\n"
"E 214,214\n"
"E 215,215\n"
"Mvaa 176,0,60\n"
"s_add_u32 m0, s84, 2048\n"
"E 216,216\n"
"E 217,217\n"
"E 218,218\n"
"E 219,219\n"
"Svav 16,20,200,254\n"
"WL 3\n"
"L 162,40,70\n"
"E 220,220\n"
"E 221,221\n"
"E 222,222\n"
"E 223,223\n"
"Nzvaa 192,26,30\n"
"s_add_u32 m0, s84, 3072\n"
"ds_read_b128 a[16:19], v225 offset:16240\n"
"E 176,176\n"
"E 177,177\n"
"E 178,178\n"
"E 179,179\n"
"Svav 0,8,208,254\n"
"ds_read_b128 a[20:23], v226 offset:16240\n"
"L 163,40,70\n"
"E 180,180\n"
"E 181,181\n"
"E 182,182\n"
"E 183,183\n"
"Mvaa 192,4,56\n"
"s_add_u32 m0, s84, 4096\n"
"ds_read_b128 a[0:3], v224 offset:18560\n"
"E 184,184\n"
"E 185,185\n"
"E 186,186\n"
"E 187,187\n"
"Svav 0,12,216,254\n"
"s_mov_b32 exec_hi, 0\n"
"L 246,40,70\n"
"s_mov_b32 exec_hi, -1\n"
"E 188,188\n"
"E 189,189\n"
"E 190,190\n"
"E 191,191\n"
"Nzvaa 208,26,34\n"
"v_add_u32 v246, 18432, v249\n"
"v_add_u32 v247, s87, v249\n"
"E 192,192\n"
"E 193,193\n"
"E 194,194\n"
"E 195,195\n"
"Svav 16,8,176,254\n"
"s_add_u32 s70, s70, 124416\n"
"buffer_load_dwordx2 v[160:161], v249, s[44:47], s73 offen\n"
"buffer_load_dwordx2 v[162:163], v246, s[44:47], s73 offen\n"
"E 196,196\n"
"E 197,197\n"
"E 198,198\n"
"E 199,199\n"
"Mvaa 208,4,60\n"
"s_add_u32 m0, s85, 18432\n"
"E 200,200\n"
"E 201,201\n"
"E 202,202\n"
"E 203,203\n"
"Svav 16,12,184,254\n"
"WL 0\n"
"L 240,40,70\n"
"E 204,204\n"
"E 205,205\n"
"E 206,206\n"
"E 207,207\n"
"Nzvaa 176,24,28\n"
"s_add_u32 m0, s85, 19456\n"
"ds_read_b128 a[8:11], v225 offset:18560\n"
"E 208,208\n"
"E 209,209\n"
"E 210,210\n"
"E 211,211\n"
"Svav 0,16,192,254\n"
"ds_read_b128 a[12:15], v226 offset:18560\n"
"L 241,40,70\n"
"E 212,212\n"
"E 213,213\n"
"E 214,214\n"
"E 215,215\n"
"Mvaa 176,0,56\n"
"s_add_u32 m0, s85, 20480\n"
"ds_read_b128 a[4:7], v224 offset:20880\n"
"buffer_load_dwordx2 v[164:165], v249, s[44:47], s73 offen offset:32\n"
"E 216,216\n"
"E 217,217\n"
"E 218,218\n"
"E 219,219\n"
"Svav 0,20,200,254\n"
"ds_read_b32 a26, v227 offset:23200\n"
"L 242,40,70\n"
"E 220,220\n"
"E 221,221\n"
"E 222,222\n"
"E 223,223\n"
"Nzvaa 192,24,32\n"
"s_add_u32 m0, s85, 21504\n"
"E 176,176\n"
"E 177,177\n"
"E 178,178\n"
"E 179,179\n"
"Svav 16,16,208,254\n"
"L 243,40,70\n"
"E 180,180\n"
"E 181,181\n"
"E 182,182\n"
"E 183,183\n"
"Mvaa 192,0,60\n"
"s_add_u32 m0, s85, 22528\n"
"buffer_load_dwordx2 v[166:167], v246, s[44:47], s73 offen offset:32\n"
"E 184,184\n"
"E 185,185\n"
"E 186,186\n"
"E 187,187\n"
"Svav 16,20,216,254\n"
"WL 1\n"
"E 188,188\n"
"E 189,189\n"
"E 190,190\n"
"E 191,191\n"
"Nzvaa 208,24,30\n"
"s_mov_b32 exec_hi, 0\n"
"L 238,40,70\n"
"s_mov_b32 exec_hi, -1\n"
"ds_read_b128 a[16:19], v225 offset:20880\n"
"E 192,192\n"
"E 193,193\n"
"E 194,194\n"
"E 195,195\n"
"Svav 0,8,176,254\n"
"s_add_u32 s70, s70, 20736\n"
"ds_read_b128 a[20:23], v226 offset:20880\n"
"E 196,196\n"
"E 197,197\n"
"E 198,198\n"
"E 199,199\n"
"Mvaa 208,4,56\n"
"s_add_u32 m0, s85, 23040\n"
"ds_read_b128 a[0:3], v224 offset:23200\n"
"E 200,200\n"
"E 201,201\n"
"E 202,202\n"
"E 203,203\n"
"Svav 0,12,184,254\n"
"L 240,40,70\n"
"buffer_load_dwordx2 v[168:169], v247, s[44:47], s73 offen\n"
"E 204,204\n"
"E 205,205\n"
"E 206,206\n"
"E 207,207\n"
"Nzvaa 176,24,34\n"
"s_add_u32 m0, s85, 24064\n"
"E 208,208\n"
"E 209,209\n"
"E 210,210\n"
"E 211,211\n"
"Svav 16,8,192,254\n"
"L 241,40,70\n"
"E 212,212\n"
"E 213,213\n"
"E 214,214\n"
"E 215,215\n"
"Mvaa 176,4,60\n"
"s_add_u32 m0, s85, 25088\n"
"E 216,216\n"
"E 217,217\n"
"E 218,218\n"
"E 219,219\n"
"Svav 16,12,200,254\n"
"WL 0\n"
"L 242,40,70\n"
"E 220,220\n"
"E 221,221\n"
"E 222,222\n"
"E 223,223\n"
"Nzvaa 192,26,28\n"
"s_add_u32 m0, s85, 26112\n"
"ds_read_b128 a[8:11], v225 offset:23200\n"
"E 176,176\n"
"E 177,177\n"
"E 178,178\n"
"E 179,179\n"
"Svav 0,16,208,254\n"
"ds_read_b128 a[12:15], v226 offset:23200\n"
"L 243,40,70\n"
"E 180,180\n"
"E 181,181\n"
"E 182,182\n"
"E 183,183\n"
"Mvaa 192,0,56\n"
"s_add_u32 m0, s85, 27136\n"
"ds_read_b128 a[4:7], v224 offset:25520\n"
"E 184,184\n"
"E 185,185\n"
"E 186,186\n"
"E 187,187\n"
"Svav 0,20,216,254\n"
"ds_read_b32 a24, v227 offset:27840\n"
"E 188,188\n"
"E 189,189\n"
"E 190,190\n"
"E 191,191\n"
"Nzvaa 208,26,32\n"
"s_barrier\n"
"buffer_load_dwordx2 v[228:229], v235, s[20:23], s78 offen offset:32 sc1\n"
"E 192,192\n"
"E 193,193\n"
"E 194,194\n"
"E 195,195\n"
"Svav 16,16,176,254\n"
"s_mov_b32 exec_hi, 0\n"
"L 238,40,70\n"
"s_mov_b32 exec_hi, -1\n"
"E 196,196\n"
"E 197,197\n"
"E 198,198\n"
"E 199,199\n"
"Mvaa 208,0,60\n"
"v_add_u32 v240, s85, v232\n"
"E 200,200\n"
"E 201,201\n"
"E 202,202\n"
"E 203,203\n"
"Svav 16,20,184,254\n"
"WL 1\n"
"E 204,204\n"
"E 205,205\n"
"E 206,206\n"
"E 207,207\n"
"Nzvaa 176,26,30\n"
"s_lshr_b32 s93, s55, 1\n"
"ds_read_b128 a[16:19], v225 offset:25520\n"
"E 208,208\n"
"E 209,209\n"
"E 210,210\n"
"E 211,211\n"
"Svav 0,8,192,254\n"
"s_mul_i32 s93, s93, 28672\n"
"ds_read_b128 a[20:23], v226 offset:25520\n"
"E 212,212\n"
"E 213,213\n"
"E 214,214\n"
"E 215,215\n"
"Mvaa 176,4,56\n"
"s_lshl_b32 s94, s55, 12\n"
"ds_read_b128 a[0:3], v224 offset:27840\n"
"E 216,216\n"
"E 217,217\n"
"E 218,218\n"
"E 219,219\n"
"Svav 0,12,200,254\n"
"s_add_u32 s93, s93, s94\n"
"v_add_u32 v241, s93, v232\n"
"E 220,220\n"
"E 221,221\n"
"E 222,222\n"
"E 223,223\n"
"Nzvaa 192,26,34\n"
"s_and_b32 s93, s55, 1\n"
"E 176,176\n"
"E 177,177\n"
"E 178,178\n"
"E 179,179\n"
"Svav 16,8,208,254\n"
"s_mul_i32 s93, s93, 36864\n"
"v_add_u32 v242, s93, v232\n"
"v_and_b32 v253, 0x380, v235\n"
"E 180,180\n"
"E 181,181\n"
"E 182,182\n"
"E 183,183\n"
"Mvaa 192,4,60\n"
"s_mov_b32 s95, 0x71800000\n"
"E 184,184\n"
"E 185,185\n"
"E 186,186\n"
"E 187,187\n"
"Svav 16,12,216,254\n"
"WL 0\n"
"E 188,188\n"
"E 189,189\n"
"E 190,190\n"
"E 191,191\n"
"Nzvaa 208,24,28\n"
"s_mov_b32 s98, 0x33800000\n"
"ds_read_b128 a[8:11], v225 offset:27840\n"
"E 192,192\n"
"E 193,193\n"
"E 194,194\n"
"E 195,195\n"
"Svav 0,16,176,254\n"
"s_mov_b32 s99, 0x40088e99\n"
"ds_read_b128 a[12:15], v226 offset:27840\n"
"E 196,196\n"
"E 197,197\n"
"E 198,198\n"
"E 199,199\n"
"Mvaa 208,0,56\n"
"ds_read_b128 a[4:7], v224 offset:30160\n"
"E 200,200\n"
"E 201,201\n"
"E 202,202\n"
"E 203,203\n"
"Svav 0,20,184,254\n"
"ds_read_b32 a26, v227 offset:32480\n"
"E 204,204\n"
"E 205,205\n"
"E 206,206\n"
"E 207,207\n"
"Nzvaa 176,24,32\n"
"WV 1\n"
"v_cmp_ne_u64 vcc, s[82:83], v[228:229]\n"
"s_cbranch_vccnz SLY%=\n"
"BKY%=:\n"
"E 208,208\n"
"E 209,209\n"
"E 210,210\n"
"E 211,211\n"
"Svav 16,16,192,254\n"
"E 212,212\n"
"E 213,213\n"
"E 214,214\n"
"E 215,215\n"
"Mvaa 176,0,60\n"
"E 216,216\n"
"E 217,217\n"
"E 218,218\n"
"E 219,219\n"
"Svav 16,20,200,254\n"
"WL 1\n"
"E 220,220\n"
"E 221,221\n"
"E 222,222\n"
"E 223,223\n"
"Nzvaa 192,24,30\n"
"ds_read_b128 a[16:19], v225 offset:30160\n"
"buffer_load_dwordx4 a[64:67], v232, s[20:23], s69 offen sc1\n"
"E 176,176\n"
"E 177,177\n"
"E 178,178\n"
"E 179,179\n"
"Svav 0,8,208,254\n"
"ds_read_b128 a[20:23], v226 offset:30160\n"
"E 180,180\n"
"E 181,181\n"
"E 182,182\n"
"E 183,183\n"
"Mvaa 192,4,56\n"
"ds_read_b128 a[0:3], v224 offset:32480\n"
"E 184,184\n"
"E 185,185\n"
"E 186,186\n"
"E 187,187\n"
"Svav 0,12,216,254\n"
"buffer_load_dwordx4 a[68:71], v232, s[20:23], s69 offen offset:1024 sc1\n"
"E 188,188\n"
"E 189,189\n"
"E 190,190\n"
"E 191,191\n"
"Nzvaa 208,24,34\n"
"E 192,192\n"
"E 193,193\n"
"E 194,194\n"
"E 195,195\n"
"Svav 16,8,176,254\n"
"E 196,196\n"
"E 197,197\n"
"E 198,198\n"
"E 199,199\n"
"Mvaa 208,4,60\n"
"buffer_load_dwordx4 a[72:75], v232, s[20:23], s86 offen sc1\n"
"E 200,200\n"
"E 201,201\n"
"E 202,202\n"
"E 203,203\n"
"Svav 16,12,184,254\n"
"WL 0\n"
"E 204,204\n"
"E 205,205\n"
"E 206,206\n"
"E 207,207\n"
"Nzvaa 176,26,28\n"
"ds_read_b128 a[8:11], v225 offset:32480\n"
"E 208,208\n"
"E 209,209\n"
"E 210,210\n"
"E 211,211\n"
"Svav 0,16,192,254\n"
"s_add_u32 s69, s69, 9216\n"
"ds_read_b128 a[12:15], v226 offset:32480\n"
"buffer_load_dwordx4 a[76:79], v232, s[20:23], s69 offen sc1\n"
"E 212,212\n"
"E 213,213\n"
"E 214,214\n"
"E 215,215\n"
"Mvaa 176,0,56\n"
"ds_read_b128 a[4:7], v224 offset:34800\n"
"E 216,216\n"
"E 217,217\n"
"E 218,218\n"
"E 219,219\n"
"Svav 0,20,200,254\n"
"ds_read_b32 a24, v227 offset:37120\n"
"E 220,220\n"
"E 221,221\n"
"E 222,222\n"
"E 223,223\n"
"Nzvaa 192,26,32\n"
"buffer_load_dwordx4 a[80:83], v232, s[20:23], s69 offen offset:1024 sc1\n"
"E 176,176\n"
"E 177,177\n"
"E 178,178\n"
"E 179,179\n"
"Svav 16,16,208,254\n"
"E 180,180\n"
"E 181,181\n"
"E 182,182\n"
"E 183,183\n"
"Mvaa 192,0,60\n"
"E 184,184\n"
"E 185,185\n"
"E 186,186\n"
"E 187,187\n"
"Svav 16,20,216,254\n"
"WL 1\n"
"E 188,188\n"
"E 189,189\n"
"E 190,190\n"
"E 191,191\n"
"Nzvaa 208,26,30\n"
"s_add_u32 s86, s86, 9216\n"
"ds_read_b128 a[16:19], v225 offset:34800\n"
"buffer_load_dwordx4 a[84:87], v232, s[20:23], s86 offen sc1\n"
"E 192,192\n"
"E 193,193\n"
"E 194,194\n"
"E 195,195\n"
"Svav 0,8,176,254\n"
"ds_read_b128 a[20:23], v226 offset:34800\n"
"E 196,196\n"
"E 197,197\n"
"E 198,198\n"
"E 199,199\n"
"Mvaa 208,4,56\n"
"s_add_u32 s69, s69, 9216\n"
"ds_read_b128 a[0:3], v224 offset:37120\n"
"buffer_load_dwordx4 a[88:91], v232, s[20:23], s69 offen sc1\n"
"E 200,200\n"
"E 201,201\n"
"E 202,202\n"
"E 203,203\n"
"Svav 0,12,184,254\n"
"E 204,204\n"
"E 205,205\n"
"E 206,206\n"
"E 207,207\n"
"Nzvaa 176,26,34\n"
"E 208,208\n"
"E 209,209\n"
"E 210,210\n"
"E 211,211\n"
"Svav 16,8,192,254\n"
"buffer_load_dwordx4 a[92:95], v232, s[20:23], s69 offen offset:1024 sc1\n"
"E 212,212\n"
"E 213,213\n"
"E 214,214\n"
"E 215,215\n"
"Mvaa 176,4,60\n"
"E 216,216\n"
"E 217,217\n"
"E 218,218\n"
"E 219,219\n"
"Svav 16,12,200,254\n"
"WL 0\n"
"E 220,220\n"
"E 221,221\n"
"E 222,222\n"
"E 223,223\n"
"Nzvaa 192,24,28\n"
"s_add_u32 s86, s86, 9216\n"
"ds_read_b128 a[8:11], v225 offset:37120\n"
"buffer_load_dwordx4 a[96:99], v232, s[20:23], s86 offen sc1\n"
"E 176,176\n"
"E 177,177\n"
"E 178,178\n"
"E 179,179\n"
"Svav 0,16,208,254\n"
"ds_read_b128 a[12:15], v226 offset:37120\n"
"E 180,180\n"
"E 181,181\n"
"E 182,182\n"
"E 183,183\n"
"Mvaa 192,0,56\n"
"ds_read_b128 a[4:7], v224 offset:39440\n"
"E 184,184\n"
"E 185,185\n"
"E 186,186\n"
"E 187,187\n"
"Svav 0,20,216,254\n"
"s_add_u32 s69, s69, 9216\n"
"ds_read_b32 a26, v227 offset:41760\n"
"buffer_load_dwordx4 a[100:103], v232, s[20:23], s69 offen sc1\n"
"E 188,188\n"
"E 189,189\n"
"E 190,190\n"
"E 191,191\n"
"Nzvaa 208,24,32\n"
"E 192,192\n"
"E 193,193\n"
"E 194,194\n"
"E 195,195\n"
"Svav 16,16,176,254\n"
"E 196,196\n"
"E 197,197\n"
"E 198,198\n"
"E 199,199\n"
"Mvaa 208,0,60\n"
"buffer_load_dwordx4 a[104:107], v232, s[20:23], s69 offen offset:1024 sc1\n"
"E 200,200\n"
"E 201,201\n"
"E 202,202\n"
"E 203,203\n"
"Svav 16,20,184,254\n"
"WL 1\n"
"E 204,204\n"
"E 205,205\n"
"E 206,206\n"
"E 207,207\n"
"Nzvaa 176,24,30\n"
"ds_read_b128 a[16:19], v225 offset:39440\n"
"E 208,208\n"
"E 209,209\n"
"E 210,210\n"
"E 211,211\n"
"Svav 0,8,192,254\n"
"s_add_u32 s86, s86, 9216\n"
"ds_read_b128 a[20:23], v226 offset:39440\n"
"buffer_load_dwordx4 a[108:111], v232, s[20:23], s86 offen sc1\n"
"E 212,212\n"
"E 213,213\n"
"E 214,214\n"
"E 215,215\n"
"Mvaa 176,4,56\n"
"ds_read_b128 a[0:3], v224 offset:41760\n"
"E 216,216\n"
"E 217,217\n"
"E 218,218\n"
"E 219,219\n"
"Svav 0,12,200,254\n"
"E 220,220\n"
"E 221,221\n"
"E 222,222\n"
"E 223,223\n"
"Nzvaa 192,24,34\n"
"s_add_u32 s69, s69, 9216\n"
"buffer_load_dwordx4 a[112:115], v232, s[20:23], s69 offen sc1\n"
"E 176,176\n"
"E 177,177\n"
"E 178,178\n"
"E 179,179\n"
"Svav 16,8,208,254\n"
"E 180,180\n"
"E 181,181\n"
"E 182,182\n"
"E 183,183\n"
"Mvaa 192,4,60\n"
"E 184,184\n"
"E 185,185\n"
"E 186,186\n"
"E 187,187\n"
"Svav 16,12,216,254\n"
"WL 0\n"
"buffer_load_dwordx4 a[116:119], v232, s[20:23], s69 offen offset:1024 sc1\n"
"E 188,188\n"
"E 189,189\n"
"E 190,190\n"
"E 191,191\n"
"Nzvaa 208,26,28\n"
"ds_read_b128 a[8:11], v225 offset:41760\n"
"E 192,192\n"
"E 193,193\n"
"E 194,194\n"
"E 195,195\n"
"Svav 0,16,176,254\n"
"ds_read_b128 a[12:15], v226 offset:41760\n"
"E 196,196\n"
"E 197,197\n"
"E 198,198\n"
"E 199,199\n"
"Mvaa 208,0,56\n"
"s_add_u32 s86, s86, 9216\n"
"ds_read_b128 a[4:7], v224 offset:44080\n"
"buffer_load_dwordx4 a[120:123], v232, s[20:23], s86 offen sc1\n"
"E 200,200\n"
"E 201,201\n"
"E 202,202\n"
"E 203,203\n"
"Svav 0,20,184,254\n"
"ds_read_b32 a24, v227 offset:46400\n"
"E 204,204\n"
"E 205,205\n"
"E 206,206\n"
"E 207,207\n"
"Nzvaa 176,26,32\n"
"E 208,208\n"
"E 209,209\n"
"E 210,210\n"
"E 211,211\n"
"Svav 16,16,192,254\n"
"s_add_u32 s69, s69, 9216\n"
"buffer_load_dwordx4 a[124:127], v232, s[20:23], s69 offen sc1\n"
"E 212,212\n"
"E 213,213\n"
"E 214,214\n"
"E 215,215\n"
"Mvaa 176,0,60\n"
"E 216,216\n"
"E 217,217\n"
"E 218,218\n"
"E 219,219\n"
"Svav 16,20,200,254\n"
"WL 1\n"
"E 220,220\n"
"E 221,221\n"
"E 222,222\n"
"E 223,223\n"
"Nzvaa 192,26,30\n"
"ds_read_b128 a[16:19], v225 offset:44080\n"
"buffer_load_dwordx4 a[128:131], v232, s[20:23], s69 offen offset:1024 sc1\n"
"E 176,176\n"
"E 177,177\n"
"E 178,178\n"
"E 179,179\n"
"Svav 0,8,208,254\n"
"ds_read_b128 a[20:23], v226 offset:44080\n"
"E 180,180\n"
"E 181,181\n"
"E 182,182\n"
"E 183,183\n"
"Mvaa 192,4,56\n"
"ds_read_b128 a[0:3], v224 offset:46400\n"
"E 184,184\n"
"E 185,185\n"
"E 186,186\n"
"E 187,187\n"
"Svav 0,12,216,254\n"
"s_add_u32 s86, s86, 9216\n"
"buffer_load_dwordx4 a[132:135], v232, s[20:23], s86 offen sc1\n"
"E 188,188\n"
"E 189,189\n"
"E 190,190\n"
"E 191,191\n"
"Nzvaa 208,26,34\n"
"E 192,192\n"
"E 193,193\n"
"E 194,194\n"
"E 195,195\n"
"Svav 16,8,176,254\n"
"E 196,196\n"
"E 197,197\n"
"E 198,198\n"
"E 199,199\n"
"Mvaa 208,4,60\n"
"s_add_u32 s69, s69, 9216\n"
"buffer_load_dwordx4 a[136:139], v232, s[20:23], s69 offen sc1\n"
"E 200,200\n"
"E 201,201\n"
"E 202,202\n"
"E 203,203\n"
"Svav 16,12,184,254\n"
"WL 0\n"
"E 204,204\n"
"E 205,205\n"
"E 206,206\n"
"E 207,207\n"
"Nzvaa 176,24,28\n"
"ds_read_b128 a[8:11], v225 offset:46400\n"
"E 208,208\n"
"E 209,209\n"
"E 210,210\n"
"E 211,211\n"
"Svav 0,16,192,254\n"
"ds_read_b128 a[12:15], v226 offset:46400\n"
"buffer_load_dwordx4 a[140:143], v232, s[20:23], s69 offen offset:1024 sc1\n"
"E 212,212\n"
"E 213,213\n"
"E 214,214\n"
"E 215,215\n"
"Mvaa 176,0,56\n"
"ds_read_b128 a[4:7], v224 offset:48720\n"
"E 216,216\n"
"E 217,217\n"
"E 218,218\n"
"E 219,219\n"
"Svav 0,20,200,254\n"
"ds_read_b32 a26, v227 offset:51040\n"
"E 220,220\n"
"E 221,221\n"
"E 222,222\n"
"E 223,223\n"
"Nzvaa 192,24,32\n"
"s_add_u32 s86, s86, 9216\n"
"buffer_load_dwordx4 a[144:147], v232, s[20:23], s86 offen sc1\n"
"E 176,176\n"
"E 177,177\n"
"E 178,178\n"
"E 179,179\n"
"Svav 16,16,208,254\n"
"E 180,180\n"
"E 181,181\n"
"E 182,182\n"
"E 183,183\n"
"Mvaa 192,0,60\n"
"E 184,184\n"
"E 185,185\n"
"E 186,186\n"
"E 187,187\n"
"Svav 16,20,216,254\n"
"WL 1\n"
"E 188,188\n"
"E 189,189\n"
"E 190,190\n"
"E 191,191\n"
"Nzvaa 208,24,30\n"
"s_add_u32 s69, s69, 9216\n"
"ds_read_b128 a[16:19], v225 offset:48720\n"
"buffer_load_dwordx4 a[148:151], v232, s[20:23], s69 offen sc1\n"
"E 192,192\n"
"E 193,193\n"
"E 194,194\n"
"E 195,195\n"
"Svav 0,8,176,254\n"
"ds_read_b128 a[20:23], v226 offset:48720\n"
"E 196,196\n"
"E 197,197\n"
"E 198,198\n"
"E 199,199\n"
"Mvaa 208,4,56\n"
"ds_read_b128 a[0:3], v224 offset:51040\n"
"buffer_load_dwordx4 a[152:155], v232, s[20:23], s69 offen offset:1024 sc1\n"
"E 200,200\n"
"E 201,201\n"
"E 202,202\n"
"E 203,203\n"
"Svav 0,12,184,254\n"
"E 204,204\n"
"E 205,205\n"
"E 206,206\n"
"E 207,207\n"
"Nzvaa 176,24,34\n"
"E 208,208\n"
"E 209,209\n"
"E 210,210\n"
"E 211,211\n"
"Svav 16,8,192,254\n"
"s_add_u32 s86, s86, 9216\n"
"buffer_load_dwordx4 a[156:159], v232, s[20:23], s86 offen sc1\n"
"E 212,212\n"
"E 213,213\n"
"E 214,214\n"
"E 215,215\n"
"Mvaa 176,4,60\n"
"E 216,216\n"
"E 217,217\n"
"E 218,218\n"
"E 219,219\n"
"Svav 16,12,200,254\n"
"WL 0\n"
"E 220,220\n"
"E 221,221\n"
"E 222,222\n"
"E 223,223\n"
"Nzvaa 192,26,28\n"
"ds_read_b128 a[8:11], v225 offset:51040\n"
"E 176,176\n"
"E 177,177\n"
"E 178,178\n"
"E 179,179\n"
"Svav 0,16,208,254\n"
"ds_read_b128 a[12:15], v226 offset:51040\n"
"E 180,180\n"
"E 181,181\n"
"E 182,182\n"
"E 183,183\n"
"Mvaa 192,0,56\n"
"ds_read_b128 a[4:7], v224 offset:53360\n"
"E 184,184\n"
"E 185,185\n"
"E 186,186\n"
"E 187,187\n"
"Svav 0,20,216,254\n"
"E 188,188\n"
"E 189,189\n"
"E 190,190\n"
"E 191,191\n"
"Nzvaa 208,26,32\n"
"E 192,192\n"
"E 193,193\n"
"E 194,194\n"
"E 195,195\n"
"Svav 16,16,176,254\n"
"E 196,196\n"
"E 197,197\n"
"E 198,198\n"
"E 199,199\n"
"Mvaa 208,0,60\n"
"E 200,200\n"
"E 201,201\n"
"E 202,202\n"
"E 203,203\n"
"Svav 16,20,184,254\n"
"WL 0\n"
"E 204,204\n"
"E 205,205\n"
"E 206,206\n"
"E 207,207\n"
"Nzvaa 176,26,30\n"
"ds_read_b128 a[16:19], v225 offset:53360\n"
"E 208,208\n"
"E 209,209\n"
"E 210,210\n"
"E 211,211\n"
"Svav 0,8,192,254\n"
"ds_read_b128 a[20:23], v226 offset:53360\n"
"E 212,212\n"
"E 213,213\n"
"E 214,214\n"
"E 215,215\n"
"Mvaa 176,4,56\n"
"E 216,216\n"
"E 217,217\n"
"E 218,218\n"
"E 219,219\n"
"Svav 0,12,200,254\n"
"E 220,220\n"
"E 221,221\n"
"E 222,222\n"
"E 223,223\n"
"Nzvaa 192,26,34\n"
"E 176,176\n"
"E 177,177\n"
"E 178,178\n"
"E 179,179\n"
"Svav 16,8,208,254\n"
"E 180,180\n"
"E 181,181\n"
"E 182,182\n"
"E 183,183\n"
"Mvaa 192,4,60\n"
"E 184,184\n"
"E 185,185\n"
"E 186,186\n"
"E 187,187\n"
"Svav 16,12,216,254\n"
"WL 0\n"
"E 188,188\n"
"E 189,189\n"
"E 190,190\n"
"E 191,191\n"
"Svav 0,16,176,254\n"
"E 192,192\n"
"E 193,193\n"
"E 194,194\n"
"E 195,195\n"
"E 196,196\n"
"E 197,197\n"
"E 198,198\n"
"E 199,199\n"
"Svav 0,20,184,254\n"
"E 200,200\n"
"E 201,201\n"
"E 202,202\n"
"E 203,203\n"
"E 204,204\n"
"E 205,205\n"
"E 206,206\n"
"E 207,207\n"
"Svav 16,16,192,254\n"
"s_nop 0\n"
"Svav 16,20,200,254\n"
"v_maximum3_f32 v12, |v0|, |v1|, |v2|\n"
"v_maximum3_f32 v13, |v3|, |v4|, |v5|\n"
"v_maximum3_f32 v12, |v12|, |v6|, |v7|\n"
"v_maximum3_f32 v13, |v13|, |v8|, |v9|\n"
"v_maximum3_f32 v12, |v12|, |v13|, |v10|\n"
"v_cmp_nle_f32 vcc, s98, v10\n"
"s_cbranch_vccnz FB0%=\n"
"v_cmp_nge_f32 vcc, s95, v12\n"
"s_cbranch_vccnz FB0%=\n"
"FR0%=:\n"
"RC 12,10\n"
"s_nop 0\n"
"MS 12,99,12\n"
"v_pk_mul_f32 v[0:1], v[0:1], v[12:13] op_sel_hi:[1,0]\n"
"v_pk_mul_f32 v[2:3], v[2:3], v[12:13] op_sel_hi:[1,0]\n"
"v_pk_mul_f32 v[4:5], v[4:5], v[12:13] op_sel_hi:[1,0]\n"
"v_pk_mul_f32 v[6:7], v[6:7], v[12:13] op_sel_hi:[1,0]\n"
"v_pk_mul_f32 v[8:9], v[8:9], v[12:13] op_sel_hi:[1,0]\n"
"CV 8,8,9\n"
"CV 0,0,1\n"
"CV 1,2,3\n"
"CV 2,4,5\n"
"CV 3,6,7\n"
"buffer_store_dwordx4 v[0:3], v232, s[20:23], s71 offen sc1\n"
"v_mov_b32_dpp v9, v8 quad_perm:[1,1,1,1] row_mask:0xf bank_mask:0xf\n"
"v_mov_b32_dpp v10, v8 quad_perm:[2,2,2,2] row_mask:0xf bank_mask:0xf\n"
"v_mov_b32_dpp v11, v8 quad_perm:[3,3,3,3] row_mask:0xf bank_mask:0xf\n"
"buffer_store_dwordx4 v[8:11], v172, s[20:23], s71 offen sc1\n"
"v_maximum3_f32 v28, |v16|, |v17|, |v18|\n"
"v_maximum3_f32 v29, |v19|, |v20|, |v21|\n"
"v_maximum3_f32 v28, |v28|, |v22|, |v23|\n"
"v_maximum3_f32 v29, |v29|, |v24|, |v25|\n"
"v_maximum3_f32 v28, |v28|, |v29|, |v26|\n"
"v_cmp_nle_f32 vcc, s98, v26\n"
"s_cbranch_vccnz FB1%=\n"
"v_cmp_nge_f32 vcc, s95, v28\n"
"s_cbranch_vccnz FB1%=\n"
"FR1%=:\n"
"RC 28,26\n"
"s_nop 0\n"
"MS 28,99,28\n"
"v_pk_mul_f32 v[16:17], v[16:17], v[28:29] op_sel_hi:[1,0]\n"
"v_pk_mul_f32 v[18:19], v[18:19], v[28:29] op_sel_hi:[1,0]\n"
"v_pk_mul_f32 v[20:21], v[20:21], v[28:29] op_sel_hi:[1,0]\n"
"v_pk_mul_f32 v[22:23], v[22:23], v[28:29] op_sel_hi:[1,0]\n"
"v_pk_mul_f32 v[24:25], v[24:25], v[28:29] op_sel_hi:[1,0]\n"
"CV 24,24,25\n"
"CV 16,16,17\n"
"CV 17,18,19\n"
"CV 18,20,21\n"
"CV 19,22,23\n"
"s_add_u32 s71, s71, 36864\n"
"buffer_store_dwordx4 v[16:19], v232, s[20:23], s71 offen sc1\n"
"v_mov_b32_dpp v25, v24 quad_perm:[1,1,1,1] row_mask:0xf bank_mask:0xf\n"
"v_mov_b32_dpp v26, v24 quad_perm:[2,2,2,2] row_mask:0xf bank_mask:0xf\n"
"v_mov_b32_dpp v27, v24 quad_perm:[3,3,3,3] row_mask:0xf bank_mask:0xf\n"
"buffer_store_dwordx4 v[24:27], v172, s[20:23], s71 offen sc1\n"
"WV 27\n"
"Gzvaa 140,184,64\n"
"Gzvaa 144,182,64\n"
"WV 26\n"
"Gzvaa 148,184,68\n"
"Gzvaa 152,182,68\n"
"WV 25\n"
"Gzvaa 156,188,72\n"
"WV 24\n"
"Gvaa 140,184,76\n"
"Gvaa 144,182,76\n"
"WV 23\n"
"Gvaa 148,184,80\n"
"Gvaa 152,182,80\n"
"WV 22\n"
"Gvaa 156,188,84\n"
"WV 21\n"
"Gvaa 140,184,88\n"
"Gvaa 144,182,88\n"
"WV 20\n"
"Gvaa 148,184,92\n"
"Gvaa 152,182,92\n"
"WV 19\n"
"Gvaa 156,188,96\n"
"WV 18\n"
"Gvaa 140,184,100\n"
"Gvaa 144,182,100\n"
"WV 17\n"
"Gvaa 148,184,104\n"
"Gvaa 152,182,104\n"
"WV 16\n"
"Gvaa 156,188,108\n"
"WV 15\n"
"Gvaa 140,184,112\n"
"Gvaa 144,182,112\n"
"WV 14\n"
"Gvaa 148,184,116\n"
"Gvaa 152,182,116\n"
"WV 13\n"
"Gvaa 156,188,120\n"
"WV 12\n"
"Gvaa 140,184,124\n"
"Gvaa 144,182,124\n"
"WV 11\n"
"Gvaa 148,184,128\n"
"Gvaa 152,182,128\n"
"WV 10\n"
"Gvaa 156,188,132\n"
"WV 9\n"
"Gvaa 140,184,136\n"
"Gvaa 144,182,136\n"
"WV 8\n"
"Gvaa 148,184,140\n"
"Gvaa 152,182,140\n"
"WV 7\n"
"Gvaa 156,188,144\n"
"WV 6\n"
"Gvaa 140,184,148\n"
"Gvaa 144,182,148\n"
"WV 5\n"
"Gvaa 148,184,152\n"
"Gvaa 152,182,152\n"
"WV 4\n"
"Gvaa 156,188,156\n"
"WV 0\n"
"buffer_store_dwordx2 v[230:231], v252, s[20:23], s76 offen offset:64 sc1\n"
"s_mov_b32 s93, 0xc0d22e3c\n"
"s_mov_b32 s94, 0x3e60ec05\n"
"Ta 164,239,0\n"
"MS 176,93,140\n"
"MS 177,93,141\n"
"Ta 166,239,32\n"
"MS 178,93,142\n"
"MS 179,93,143\n"
"Ta 168,239,64\n"
"MS 180,93,144\n"
"MS 181,93,145\n"
"Ta 170,239,96\n"
"MS 182,93,146\n"
"MS 183,93,147\n"
"Ta 172,239,128\n"
"MS 184,93,148\n"
"MS 185,93,149\n"
"Ta 174,239,160\n"
"MS 186,93,150\n"
"MS 187,93,151\n"
"Ta 176,239,192\n"
"MS 188,93,152\n"
"MS 189,93,153\n"
"Ta 178,239,224\n"
"MS 190,93,154\n"
"MS 191,93,155\n"
"Ta 180,239,256\n"
"MS 192,93,156\n"
"MS 193,93,157\n"
"MS 194,93,158\n"
"MS 195,93,159\n"
"Tv 68,237,55296\n"
"E 176,176\n"
"E 177,177\n"
"Tv 70,237,57600\n"
"E 178,178\n"
"E 179,179\n"
"Tv 72,237,55328\n"
"E 180,180\n"
"E 181,181\n"
"Tv 74,237,57632\n"
"E 182,182\n"
"E 183,183\n"
"Tv 76,237,55360\n"
"E 184,184\n"
"E 185,185\n"
"Tv 78,237,57664\n"
"E 186,186\n"
"E 187,187\n"
"Tv 80,237,55392\n"
"E 188,188\n"
"E 189,189\n"
"Tv 82,237,57696\n"
"E 190,190\n"
"E 191,191\n"
"Tv 84,237,55424\n"
"E 192,192\n"
"E 193,193\n"
"Tv 86,237,57728\n"
"E 194,194\n"
"E 195,195\n"
"Tv 88,237,55456\n"
"v_fma_f32 v176, v176, s94, s94\n"
"v_fma_f32 v177, v177, s94, s94\n"
"Tv 90,237,57760\n"
"v_fma_f32 v178, v178, s94, s94\n"
"v_fma_f32 v179, v179, s94, s94\n"
"Tv 92,237,55488\n"
"v_fma_f32 v180, v180, s94, s94\n"
"v_fma_f32 v181, v181, s94, s94\n"
"Tv 94,237,57792\n"
"v_fma_f32 v182, v182, s94, s94\n"
"v_fma_f32 v183, v183, s94, s94\n"
"Tv 96,237,55520\n"
"v_fma_f32 v184, v184, s94, s94\n"
"v_fma_f32 v185, v185, s94, s94\n"
"Tv 98,237,57824\n"
"v_fma_f32 v186, v186, s94, s94\n"
"v_fma_f32 v187, v187, s94, s94\n"
"Tv 100,237,55552\n"
"v_fma_f32 v188, v188, s94, s94\n"
"v_fma_f32 v189, v189, s94, s94\n"
"Tv 102,237,57856\n"
"v_fma_f32 v190, v190, s94, s94\n"
"v_fma_f32 v191, v191, s94, s94\n"
"v_fma_f32 v192, v192, s94, s94\n"
"v_fma_f32 v193, v193, s94, s94\n"
"Tv 104,237,18432\n"
"v_fma_f32 v194, v194, s94, s94\n"
"v_fma_f32 v195, v195, s94, s94\n"
"Tv 106,237,20736\n"
"RC 176,176\n"
"RC 177,177\n"
"Tv 108,237,18464\n"
"RC 178,178\n"
"RC 179,179\n"
"Tv 110,237,20768\n"
"RC 180,180\n"
"RC 181,181\n"
"Tv 112,237,18496\n"
"RC 182,182\n"
"RC 183,183\n"
"Tv 114,237,20800\n"
"RC 184,184\n"
"RC 185,185\n"
"Tv 116,237,18528\n"
"RC 186,186\n"
"RC 187,187\n"
"Tv 118,237,20832\n"
"RC 188,188\n"
"RC 189,189\n"
"Tv 120,237,18560\n"
"RC 190,190\n"
"RC 191,191\n"
"Tv 122,237,20864\n"
"RC 192,192\n"
"RC 193,193\n"
"Tv 124,237,18592\n"
"RC 194,194\n"
"RC 195,195\n"
"Tv 126,237,20896\n"
"v_pk_mul_f32 v[140:141], v[140:141], v[176:177]\n"
"v_pk_mul_f32 v[142:143], v[142:143], v[178:179]\n"
"Tv 128,237,18624\n"
"v_pk_mul_f32 v[144:145], v[144:145], v[180:181]\n"
"v_pk_mul_f32 v[146:147], v[146:147], v[182:183]\n"
"Tv 130,237,20928\n"
"v_pk_mul_f32 v[148:149], v[148:149], v[184:185]\n"
"v_pk_mul_f32 v[150:151], v[150:151], v[186:187]\n"
"Tv 132,237,18656\n"
"v_pk_mul_f32 v[152:153], v[152:153], v[188:189]\n"
"v_pk_mul_f32 v[154:155], v[154:155], v[190:191]\n"
"Tv 134,237,20960\n"
"v_pk_mul_f32 v[156:157], v[156:157], v[192:193]\n"
"v_pk_mul_f32 v[158:159], v[158:159], v[194:195]\n"
"Tv 136,237,18688\n"
"Tv 138,237,20992\n"
"v_lshlrev_b32 v196, 16, v160\n"
"v_and_b32 v197, 0xffff0000, v160\n"
"v_lshlrev_b32 v198, 16, v161\n"
"v_and_b32 v199, 0xffff0000, v161\n"
"v_lshlrev_b32 v200, 16, v162\n"
"v_and_b32 v201, 0xffff0000, v162\n"
"v_lshlrev_b32 v202, 16, v163\n"
"v_and_b32 v203, 0xffff0000, v163\n"
"v_lshlrev_b32 v204, 16, v164\n"
"v_and_b32 v205, 0xffff0000, v164\n"
"v_lshlrev_b32 v206, 16, v165\n"
"v_and_b32 v207, 0xffff0000, v165\n"
"v_lshlrev_b32 v208, 16, v166\n"
"v_and_b32 v209, 0xffff0000, v166\n"
"v_lshlrev_b32 v210, 16, v167\n"
"v_and_b32 v211, 0xffff0000, v167\n"
"v_lshlrev_b32 v212, 16, v168\n"
"v_and_b32 v213, 0xffff0000, v168\n"
"v_lshlrev_b32 v214, 16, v169\n"
"v_and_b32 v215, 0xffff0000, v169\n"
"buffer_load_dwordx2 v[228:229], v253, s[20:23], s81 offen offset:64 sc1\n"
"WV 0\n"
"v_cmp_ne_u64 vcc, s[82:83], v[228:229]\n"
"s_cbranch_vccnz SLO%=\n"
"BKO%=:\n"
"buffer_load_dwordx4 a[192:195], v170, s[20:23], s72 offen sc1\n"
"buffer_load_dwordx4 a[196:199], v170, s[20:23], s72 offen offset:256 sc1\n"
"buffer_load_dwordx4 a[200:203], v170, s[20:23], s72 offen offset:2304 sc1\n"
"buffer_load_dwordx4 a[204:207], v170, s[20:23], s72 offen offset:2560 sc1\n"
"buffer_load_dword a136, v171, s[20:23], s72 offen sc1\n"
"buffer_load_dword a137, v171, s[20:23], s72 offen offset:1152 sc1\n"
"buffer_load_dword a138, v171, s[20:23], s72 offen offset:64 sc1\n"
"buffer_load_dword a139, v171, s[20:23], s72 offen offset:1216 sc1\n"
"s_add_u32 s72, s72, 4608\n"
"buffer_load_dwordx4 a[208:211], v170, s[20:23], s72 offen sc1\n"
"buffer_load_dwordx4 a[212:215], v170, s[20:23], s72 offen offset:256 sc1\n"
"buffer_load_dwordx4 a[216:219], v170, s[20:23], s72 offen offset:2304 sc1\n"
"buffer_load_dwordx4 a[220:223], v170, s[20:23], s72 offen offset:2560 sc1\n"
"WV 11\n"
"Gzaaa 64,224,192\n"
"Gzaaa 72,228,192\n"
"Gzaaa 80,232,192\n"
"Gzaaa 88,236,192\n"
"Gzaaa 96,240,192\n"
"Gzaaa 104,244,192\n"
"Gzaaa 112,248,192\n"
"Gzaaa 120,252,192\n"
"Gzaaa 128,160,192\n"
"WV 10\n"
"Gzaaa 68,224,196\n"
"Gzaaa 76,228,196\n"
"Gzaaa 84,232,196\n"
"Gzaaa 92,236,196\n"
"Gzaaa 100,240,196\n"
"Gzaaa 108,244,196\n"
"Gzaaa 116,248,196\n"
"Gzaaa 124,252,196\n"
"Gzaaa 132,160,196\n"
"WV 9\n"
"Gava 64,32,200\n"
"Gava 72,36,200\n"
"Gava 80,40,200\n"
"Gava 88,44,200\n"
"Gava 96,48,200\n"
"Gava 104,52,200\n"
"Gava 112,56,200\n"
"Gava 120,60,200\n"
"Gava 128,64,200\n"
"WV 8\n"
"Gava 68,32,204\n"
"Gava 76,36,204\n"
"Gava 84,40,204\n"
"Gava 92,44,204\n"
"Gava 100,48,204\n"
"Gava 108,52,204\n"
"Gava 116,56,204\n"
"Gava 124,60,204\n"
"Gava 132,64,204\n"
"s_waitcnt vmcnt(6) lgkmcnt(15)\n"
"Kaaa 64,164,136\n"
"Kaaa 72,166,136\n"
"Kaaa 80,168,136\n"
"Kaaa 88,170,136\n"
"Kaaa 96,172,136\n"
"Kaaa 104,174,136\n"
"Kaaa 112,176,136\n"
"Kaaa 120,178,136\n"
"Kaaa 128,180,136\n"
"WV 4\n"
"Kaaa 68,164,138\n"
"Kaaa 76,166,138\n"
"Kaaa 84,168,138\n"
"Kaaa 92,170,138\n"
"Kaaa 100,172,138\n"
"Kaaa 108,174,138\n"
"Kaaa 116,176,138\n"
"Kaaa 124,178,138\n"
"Kaaa 132,180,138\n"
"WV 3\n"
"Gava 64,68,208\n"
"WV 1\n"
"Gava 64,104,216\n"
"Gava 68,68,212\n"
"WV 0\n"
"Gava 68,104,220\n"
"Gava 72,72,208\n"
"WL 14\n"
"Gava 72,108,216\n"
"Gava 76,72,212\n"
"Gava 76,108,220\n"
"ds_write_b128 v240, a[64:67] offset:18432\n"
"Gava 80,76,208\n"
"WL 13\n"
"Gava 80,112,216\n"
"ds_write_b128 v240, a[68:71] offset:19456\n"
"Gava 84,76,212\n"
"Gava 84,112,220\n"
"ds_write_b128 v240, a[72:75] offset:20480\n"
"Gava 88,80,208\n"
"WL 13\n"
"Gava 88,116,216\n"
"ds_write_b128 v240, a[76:79] offset:21504\n"
"Gava 92,80,212\n"
"Gava 92,116,220\n"
"ds_write_b128 v240, a[80:83] offset:22528\n"
"Gava 96,84,208\n"
"WL 13\n"
"Gava 96,120,216\n"
"ds_write_b128 v240, a[84:87] offset:23552\n"
"Gava 100,84,212\n"
"Gava 100,120,220\n"
"ds_write_b128 v240, a[88:91] offset:24576\n"
"Gava 104,88,208\n"
"WL 13\n"
"Gava 104,124,216\n"
"ds_write_b128 v240, a[92:95] offset:25600\n"
"Gava 108,88,212\n"
"Gava 108,124,220\n"
"ds_write_b128 v240, a[96:99] offset:55296\n"
"Gava 112,92,208\n"
"WL 13\n"
"Gava 112,128,216\n"
"ds_write_b128 v240, a[100:103] offset:56320\n"
"Gava 116,92,212\n"
"Gava 116,128,220\n"
"ds_write_b128 v240, a[104:107] offset:57344\n"
"Gava 120,96,208\n"
"WL 13\n"
"Gava 120,132,216\n"
"ds_write_b128 v240, a[108:111] offset:58368\n"
"Gava 124,96,212\n"
"Gava 124,132,220\n"
"ds_write_b128 v240, a[112:115] offset:59392\n"
"Gava 128,100,208\n"
"WL 13\n"
"Gava 128,136,216\n"
"ds_write_b128 v240, a[116:119] offset:60416\n"
"Gava 132,100,212\n"
"Gava 132,136,220\n"
"ds_write_b128 v240, a[120:123] offset:61440\n"
"ds_write_b128 v240, a[124:127] offset:62464\n"
"s_nop 2\n"
"ds_write_b128 v240, a[128:131] offset:26624\n"
"s_nop 1\n"
"ds_write_b128 v240, a[132:135] offset:63488\n"
"WL 0\n"
"s_barrier\n"
"ds_read_b128 v[32:35], v241 offset:18432\n"
"ds_read_b128 v[36:39], v241 offset:27648\n"
"ds_read_b128 v[40:43], v241 offset:36864\n"
"ds_read_b128 v[44:47], v241 offset:46080\n"
"ds_read_b128 v[48:51], v241 offset:19456\n"
"ds_read_b128 v[52:55], v241 offset:28672\n"
"ds_read_b128 v[56:59], v241 offset:37888\n"
"ds_read_b128 v[60:63], v241 offset:47104\n"
"ds_read_b128 v[64:67], v241 offset:20480\n"
"ds_read_b128 v[68:71], v241 offset:29696\n"
"ds_read_b128 v[72:75], v241 offset:38912\n"
"ds_read_b128 v[76:79], v241 offset:48128\n"
"ds_read_b128 v[80:83], v241 offset:21504\n"
"ds_read_b128 v[84:87], v241 offset:30720\n"
"ds_read_b128 v[88:91], v241 offset:39936\n"
"ds_read_b128 v[92:95], v241 offset:49152\n"
"ds_read_b128 v[96:99], v242 offset:26624\n"
"ds_read_b128 v[100:103], v242 offset:35840\n"
"ds_read_b128 v[104:107], v242 offset:45056\n"
"ds_read_b128 v[108:111], v242 offset:54272\n"
"s_cmp_lg_u32 s74, 4096\n"
"s_cbranch_scc1 NE%=\n"
"buffer_store_dwordx2 v[230:231], v252, s[20:23], 0 offen sc1\n"
"NE%=:\n"
"WL 15\n"
"v_pk_add_f32 v[32:33], v[32:33], v[36:37]\n"
"v_pk_add_f32 v[34:35], v[34:35], v[38:39]\n"
"v_pk_add_f32 v[40:41], v[40:41], v[44:45]\n"
"v_pk_add_f32 v[42:43], v[42:43], v[46:47]\n"
"v_pk_add_f32 v[32:33], v[32:33], v[40:41]\n"
"v_pk_add_f32 v[34:35], v[34:35], v[42:43]\n"
"v_pk_fma_f32 v[32:33], v[32:33], v[140:141], v[196:197]\n"
"v_pk_fma_f32 v[34:35], v[34:35], v[142:143], v[198:199]\n"
"CV 32,32,33\n"
"CV 33,34,35\n"
"buffer_store_dwordx2 v[32:33], v249, s[48:51], s73 offen\n"
"WL 14\n"
"v_pk_add_f32 v[48:49], v[48:49], v[52:53]\n"
"v_pk_add_f32 v[50:51], v[50:51], v[54:55]\n"
"WL 12\n"
"v_pk_add_f32 v[56:57], v[56:57], v[60:61]\n"
"v_pk_add_f32 v[58:59], v[58:59], v[62:63]\n"
"v_pk_add_f32 v[48:49], v[48:49], v[56:57]\n"
"v_pk_add_f32 v[50:51], v[50:51], v[58:59]\n"
"v_pk_fma_f32 v[48:49], v[48:49], v[144:145], v[200:201]\n"
"v_pk_fma_f32 v[50:51], v[50:51], v[146:147], v[202:203]\n"
"CV 48,48,49\n"
"CV 49,50,51\n"
"buffer_store_dwordx2 v[48:49], v246, s[48:51], s73 offen\n"
"WL 10\n"
"v_pk_add_f32 v[64:65], v[64:65], v[68:69]\n"
"v_pk_add_f32 v[66:67], v[66:67], v[70:71]\n"
"WL 8\n"
"v_pk_add_f32 v[72:73], v[72:73], v[76:77]\n"
"v_pk_add_f32 v[74:75], v[74:75], v[78:79]\n"
"v_pk_add_f32 v[64:65], v[64:65], v[72:73]\n"
"v_pk_add_f32 v[66:67], v[66:67], v[74:75]\n"
"v_pk_fma_f32 v[64:65], v[64:65], v[148:149], v[204:205]\n"
"v_pk_fma_f32 v[66:67], v[66:67], v[150:151], v[206:207]\n"
"CV 64,64,65\n"
"CV 65,66,67\n"
"buffer_store_dwordx2 v[64:65], v249, s[48:51], s73 offen offset:32\n"
"WL 6\n"
"v_pk_add_f32 v[80:81], v[80:81], v[84:85]\n"
"v_pk_add_f32 v[82:83], v[82:83], v[86:87]\n"
"WL 4\n"
"v_pk_add_f32 v[88:89], v[88:89], v[92:93]\n"
"v_pk_add_f32 v[90:91], v[90:91], v[94:95]\n"
"v_pk_add_f32 v[80:81], v[80:81], v[88:89]\n"
"v_pk_add_f32 v[82:83], v[82:83], v[90:91]\n"
"v_pk_fma_f32 v[80:81], v[80:81], v[152:153], v[208:209]\n"
"v_pk_fma_f32 v[82:83], v[82:83], v[154:155], v[210:211]\n"
"CV 80,80,81\n"
"CV 81,82,83\n"
"buffer_store_dwordx2 v[80:81], v246, s[48:51], s73 offen offset:32\n"
"WL 2\n"
"v_pk_add_f32 v[96:97], v[96:97], v[100:101]\n"
"v_pk_add_f32 v[98:99], v[98:99], v[102:103]\n"
"WL 0\n"
"v_pk_add_f32 v[104:105], v[104:105], v[108:109]\n"
"v_pk_add_f32 v[106:107], v[106:107], v[110:111]\n"
"v_pk_add_f32 v[96:97], v[96:97], v[104:105]\n"
"v_pk_add_f32 v[98:99], v[98:99], v[106:107]\n"
"v_pk_fma_f32 v[96:97], v[96:97], v[156:157], v[212:213]\n"
"v_pk_fma_f32 v[98:99], v[98:99], v[158:159], v[214:215]\n"
"CV 96,96,97\n"
"CV 97,98,99\n"
"buffer_store_dwordx2 v[96:97], v247, s[48:51], s73 offen\n"
"s_branch DONE%=\n"
"SLX%=:\n"
"s_cmp_eq_u32 s89, 0\n"
"s_cbranch_scc1 BKX%=\n"
"s_sub_u32 s89, s89, 1\n"
"buffer_load_dwordx2 v[228:229], v235, s[20:23], s77 offen sc1\n"
"s_waitcnt vmcnt(0)\n"
"v_cmp_ne_u64 vcc, s[82:83], v[228:229]\n"
"s_cbranch_vccnz SLX%=\n"
"s_branch BKX%=\n"
"SLS%=:\n"
"s_cmp_eq_u32 s89, 0\n"
"s_cbranch_scc1 BKS%=\n"
"s_sub_u32 s89, s89, 1\n"
"buffer_load_dwordx2 v[228:229], v241, s[20:23], s93 offen sc1\n"
"s_waitcnt vmcnt(0)\n"
"v_cmp_ne_u64 vcc, s[82:83], v[228:229]\n"
"s_cbranch_vccnz SLS%=\n"
"s_branch BKS%=\n"
"SLH%=:\n"
"s_cmp_eq_u32 s89, 0\n"
"s_cbranch_scc1 BKH%=\n"
"s_sub_u32 s89, s89, 1\n"
"buffer_load_dwordx2 v[228:229], v253, s[20:23], s80 offen sc1\n"
"s_waitcnt vmcnt(0)\n"
"v_cmp_ne_u64 vcc, s[82:83], v[228:229]\n"
"s_cbranch_vccnz SLH%=\n"
"s_branch BKH%=\n"
"SLK%=:\n"
"s_cmp_eq_u32 s89, 0\n"
"s_cbranch_scc1 BKK%=\n"
"s_sub_u32 s89, s89, 1\n"
"buffer_load_dwordx2 v[228:229], v235, s[20:23], s79 offen offset:32 sc1\n"
"s_waitcnt vmcnt(0)\n"
"v_cmp_ne_u64 vcc, s[82:83], v[228:229]\n"
"s_cbranch_vccnz SLK%=\n"
"s_branch BKK%=\n"
"SLI%=:\n"
"s_cmp_eq_u32 s89, 0\n"
"s_cbranch_scc1 BKI%=\n"
"s_sub_u32 s89, s89, 1\n"
"buffer_load_dwordx2 v[228:229], v253, s[20:23], s80 offen offset:32 sc1\n"
"s_waitcnt vmcnt(0)\n"
"v_cmp_ne_u64 vcc, s[82:83], v[228:229]\n"
"s_cbranch_vccnz SLI%=\n"
"s_branch BKI%=\n"
"SLY%=:\n"
"s_cmp_eq_u32 s89, 0\n"
"s_cbranch_scc1 BKY%=\n"
"s_sub_u32 s89, s89, 1\n"
"buffer_load_dwordx2 v[228:229], v235, s[20:23], s78 offen offset:32 sc1\n"
"s_waitcnt vmcnt(0)\n"
"v_cmp_ne_u64 vcc, s[82:83], v[228:229]\n"
"s_cbranch_vccnz SLY%=\n"
"s_branch BKY%=\n"
"FB0%=:\n"
"s_waitcnt vmcnt(0) lgkmcnt(0)\n"
"s_nop 15\n"
"s_nop 15\n"
"v_lshrrev_b32 v194, 1, v233\n"
"v_add_u32 v196, 4294875136, v225\n"
"v_add_u32 v197, 4294875136, v226\n"
"v_mov_b32 v192, 0xff800000\n"
"s_mov_b32 s93, s88\n"
"s_mov_b32 s94, 64\n"
"FA0%=:\n"
"buffer_load_dwordx4 a[0:3], v232, s[20:23], s93 offen sc1\n"
"buffer_load_dword a24, v194, s[20:23], s93 offen offset:2048 sc1\n"
"WV 0\n"
"Nzvaa 176,24,28\n"
"s_nop 9\n"
"Mvaa 176,0,56\n"
"s_nop 11\n"
"v_max3_f32 v192, v192, v176, v177\n"
"v_max3_f32 v192, v192, v178, v179\n"
"v_max3_f32 v192, v192, v180, v181\n"
"v_max3_f32 v192, v192, v182, v183\n"
"v_max3_f32 v192, v192, v184, v185\n"
"v_max3_f32 v192, v192, v186, v187\n"
"v_max3_f32 v192, v192, v188, v189\n"
"v_max3_f32 v192, v192, v190, v191\n"
"s_add_u32 s93, s93, 2304\n"
"s_sub_u32 s94, s94, 1\n"
"s_cmp_lg_u32 s94, 0\n"
"s_nop 15\n"
"s_cbranch_scc1 FA0%=\n"
"v_mov_b32 v193, v192\n"
"s_nop 1\n"
"v_permlane32_swap_b32 v192, v193\n"
"v_max_f32 v192, v192, v193\n"
"v_mov_b32 v0, 0\n"
"v_mov_b32 v1, 0\n"
"v_mov_b32 v2, 0\n"
"v_mov_b32 v3, 0\n"
"v_mov_b32 v4, 0\n"
"v_mov_b32 v5, 0\n"
"v_mov_b32 v6, 0\n"
"v_mov_b32 v7, 0\n"
"v_mov_b32 v8, 0\n"
"v_mov_b32 v9, 0\n"
"v_mov_b32 v10, 0\n"
"v_mov_b32 v11, 0\n"
"v_mov_b32 v12, 0\n"
"v_mov_b32 v13, 0\n"
"v_mov_b32 v14, 0\n"
"v_mov_b32 v15, 0\n"
"s_mov_b32 s93, s88\n"
"s_mov_b32 s94, 64\n"
"FC0%=:\n"
"buffer_load_dwordx4 a[0:3], v232, s[20:23], s93 offen sc1\n"
"buffer_load_dword a24, v194, s[20:23], s93 offen offset:2048 sc1\n"
"s_mov_b64 exec, s[90:91]\n"
"buffer_load_dwordx4 a[8:11], v196, s[20:23], s93 offen sc1\n"
"s_mov_b64 exec, -1\n"
"s_mov_b64 exec, s[90:91]\n"
"buffer_load_dwordx4 a[12:15], v197, s[20:23], s93 offen sc1\n"
"s_mov_b64 exec, -1\n"
"WV 2\n"
"Nzvaa 176,24,28\n"
"s_nop 9\n"
"Mvaa 176,0,56\n"
"s_nop 11\n"
"v_sub_f32 v176, v176, v192\n"
"v_sub_f32 v177, v177, v192\n"
"v_sub_f32 v178, v178, v192\n"
"v_sub_f32 v179, v179, v192\n"
"v_sub_f32 v180, v180, v192\n"
"v_sub_f32 v181, v181, v192\n"
"v_sub_f32 v182, v182, v192\n"
"v_sub_f32 v183, v183, v192\n"
"v_sub_f32 v184, v184, v192\n"
"v_sub_f32 v185, v185, v192\n"
"v_sub_f32 v186, v186, v192\n"
"v_sub_f32 v187, v187, v192\n"
"v_sub_f32 v188, v188, v192\n"
"v_sub_f32 v189, v189, v192\n"
"v_sub_f32 v190, v190, v192\n"
"v_sub_f32 v191, v191, v192\n"
"E 176,176\n"
"E 177,177\n"
"E 178,178\n"
"E 179,179\n"
"E 180,180\n"
"E 181,181\n"
"E 182,182\n"
"E 183,183\n"
"E 184,184\n"
"E 185,185\n"
"E 186,186\n"
"E 187,187\n"
"E 188,188\n"
"E 189,189\n"
"E 190,190\n"
"E 191,191\n"
"WV 1\n"
"Svav 0,8,176,254\n"
"WV 0\n"
"Svav 0,12,184,254\n"
"s_add_u32 s93, s93, 2304\n"
"s_sub_u32 s94, s94, 1\n"
"s_cmp_lg_u32 s94, 0\n"
"s_nop 15\n"
"s_cbranch_scc1 FC0%=\n"
"s_nop 15\n"
"s_branch FR0%=\n"
"FB1%=:\n"
"s_waitcnt vmcnt(0) lgkmcnt(0)\n"
"s_nop 15\n"
"s_nop 15\n"
"v_lshrrev_b32 v194, 1, v233\n"
"v_add_u32 v196, 4294875136, v225\n"
"v_add_u32 v197, 4294875136, v226\n"
"v_mov_b32 v192, 0xff800000\n"
"s_mov_b32 s93, s88\n"
"s_mov_b32 s94, 64\n"
"FA1%=:\n"
"buffer_load_dwordx4 a[0:3], v232, s[20:23], s93 offen sc1\n"
"buffer_load_dword a24, v194, s[20:23], s93 offen offset:2048 sc1\n"
"WV 0\n"
"Nzvaa 176,24,32\n"
"s_nop 9\n"
"Mvaa 176,0,60\n"
"s_nop 11\n"
"v_max3_f32 v192, v192, v176, v177\n"
"v_max3_f32 v192, v192, v178, v179\n"
"v_max3_f32 v192, v192, v180, v181\n"
"v_max3_f32 v192, v192, v182, v183\n"
"v_max3_f32 v192, v192, v184, v185\n"
"v_max3_f32 v192, v192, v186, v187\n"
"v_max3_f32 v192, v192, v188, v189\n"
"v_max3_f32 v192, v192, v190, v191\n"
"s_add_u32 s93, s93, 2304\n"
"s_sub_u32 s94, s94, 1\n"
"s_cmp_lg_u32 s94, 0\n"
"s_nop 15\n"
"s_cbranch_scc1 FA1%=\n"
"v_mov_b32 v193, v192\n"
"s_nop 1\n"
"v_permlane32_swap_b32 v192, v193\n"
"v_max_f32 v192, v192, v193\n"
"v_mov_b32 v16, 0\n"
"v_mov_b32 v17, 0\n"
"v_mov_b32 v18, 0\n"
"v_mov_b32 v19, 0\n"
"v_mov_b32 v20, 0\n"
"v_mov_b32 v21, 0\n"
"v_mov_b32 v22, 0\n"
"v_mov_b32 v23, 0\n"
"v_mov_b32 v24, 0\n"
"v_mov_b32 v25, 0\n"
"v_mov_b32 v26, 0\n"
"v_mov_b32 v27, 0\n"
"v_mov_b32 v28, 0\n"
"v_mov_b32 v29, 0\n"
"v_mov_b32 v30, 0\n"
"v_mov_b32 v31, 0\n"
"s_mov_b32 s93, s88\n"
"s_mov_b32 s94, 64\n"
"FC1%=:\n"
"buffer_load_dwordx4 a[0:3], v232, s[20:23], s93 offen sc1\n"
"buffer_load_dword a24, v194, s[20:23], s93 offen offset:2048 sc1\n"
"s_mov_b64 exec, s[90:91]\n"
"buffer_load_dwordx4 a[8:11], v196, s[20:23], s93 offen sc1\n"
"s_mov_b64 exec, -1\n"
"s_mov_b64 exec, s[90:91]\n"
"buffer_load_dwordx4 a[12:15], v197, s[20:23], s93 offen sc1\n"
"s_mov_b64 exec, -1\n"
"WV 2\n"
"Nzvaa 176,24,32\n"
"s_nop 9\n"
"Mvaa 176,0,60\n"
"s_nop 11\n"
"v_sub_f32 v176, v176, v192\n"
"v_sub_f32 v177, v177, v192\n"
"v_sub_f32 v178, v178, v192\n"
"v_sub_f32 v179, v179, v192\n"
"v_sub_f32 v180, v180, v192\n"
"v_sub_f32 v181, v181, v192\n"
"v_sub_f32 v182, v182, v192\n"
"v_sub_f32 v183, v183, v192\n"
"v_sub_f32 v184, v184, v192\n"
"v_sub_f32 v185, v185, v192\n"
"v_sub_f32 v186, v186, v192\n"
"v_sub_f32 v187, v187, v192\n"
"v_sub_f32 v188, v188, v192\n"
"v_sub_f32 v189, v189, v192\n"
"v_sub_f32 v190, v190, v192\n"
"v_sub_f32 v191, v191, v192\n"
"E 176,176\n"
"E 177,177\n"
"E 178,178\n"
"E 179,179\n"
"E 180,180\n"
"E 181,181\n"
"E 182,182\n"
"E 183,183\n"
"E 184,184\n"
"E 185,185\n"
"E 186,186\n"
"E 187,187\n"
"E 188,188\n"
"E 189,189\n"
"E 190,190\n"
"E 191,191\n"
"WV 1\n"
"Svav 16,8,176,254\n"
"WV 0\n"
"Svav 16,12,184,254\n"
"s_add_u32 s93, s93, 2304\n"
"s_sub_u32 s94, s94, 1\n"
"s_cmp_lg_u32 s94, 0\n"
"s_nop 15\n"
"s_cbranch_scc1 FC1%=\n"
"s_nop 15\n"
"s_branch FR1%=\n"
"SLO%=:\n"
"s_cmp_eq_u32 s89, 0\n"
"s_cbranch_scc1 BKO%=\n"
"s_sub_u32 s89, s89, 1\n"
"buffer_load_dwordx2 v[228:229], v253, s[20:23], s81 offen offset:64 sc1\n"
"s_waitcnt vmcnt(0)\n"
"v_cmp_ne_u64 vcc, s[82:83], v[228:229]\n"
"s_cbranch_vccnz SLO%=\n"
"s_branch BKO%=\n"
".Lend:\n"
"DONE%=:\n"
".purgem RC\n"
".purgem MU\n"
".purgem MS\n"
".purgem FS\n"
".purgem A1\n"
".purgem CV\n"
".purgem WL\n"
".purgem WV\n"
".purgem L\n"
".purgem Gzvva\n"
".purgem Gvva\n"
".purgem Gvvv\n"
".purgem Gzvav\n"
".purgem Gvav\n"
".purgem Gzvvv\n"
".purgem Nzvaa\n"
".purgem Mvaa\n"
".purgem Gzvaa\n"
".purgem Gvaa\n"
".purgem E\n"
".purgem Svav\n"
".purgem Tv\n"
".purgem Ta\n"
".purgem EN\n"
".purgem Gzaaa\n"
".purgem Gava\n"
".purgem Kaaa\n"
      :
      : "{v[160:163]}"(bld), "{v[230:231]}"(ev), "{v[232:235]}"(vl), "{v236}"(tru), "{v239}"(gu), "{v[240:243]}"(pf), "{v[244:245]}"(kw), "{v[248:251]}"(sg), "{v252}"(vfl),
        "{s[4:5]}"(sP), "{s[8:15]}"(sX), "{s[16:31]}"(dA), "{s[36:51]}"(dB), "{s[52:67]}"(sC), "{s[68:83]}"(sD), "{s[84:87]}"(sF), "{s[88:89]}"(sG)
      : "v0","v1","v2","v3","v4","v5","v6","v7","v8","v9","v10","v11","v12","v13","v14","v15","v16","v17","v18","v19","v20","v21","v22","v23","v24","v25","v26","v27","v28","v29","v30","v31","v32","v33","v34","v35","v36","v37","v38","v39","v40","v41","v42","v43","v44","v45","v46","v47","v48","v49","v50","v51","v52","v53","v54","v55","v56","v57","v58","v59","v60","v61","v62","v63","v64","v65","v66","v67","v68","v69","v70","v71","v72","v73","v74","v75","v76","v77","v78","v79","v80","v81","v82","v83","v84","v85","v86","v87","v88","v89","v90","v91","v92","v93","v94","v95","v96","v97","v98","v99","v100","v101","v102","v103","v104","v105","v106","v107","v108","v109","v110","v111","v112","v113","v114","v115","v116","v117","v118","v119","v120","v121","v122","v123","v124","v125","v126","v127","v128","v129","v130","v131","v132","v133","v134","v135","v136","v137","v138","v139","v140","v141","v142","v143","v144","v145","v146","v147","v148","v149","v150","v151","v152","v153","v154","v155","v156","v157","v158","v159","v164","v165","v166","v167","v168","v169","v170","v171","v172","v173","v174","v175","v176","v177","v178","v179","v180","v181","v182","v183","v184","v185","v186","v187","v188","v189","v190","v191","v192","v193","v194","v195","v196","v197","v198","v199","v200","v201","v202","v203","v204","v205","v206","v207","v208","v209","v210","v211","v212","v213","v214","v215","v216","v217","v218","v219","v220","v221","v222","v223","v224","v225","v226","v227","v228","v229","v237","v238","v246","v247","v253","v254","v255","a0","a1","a2","a3","a4","a5","a6","a7","a8","a9","a10","a11","a12","a13","a14","a15","a16","a17","a18","a19","a20","a21","a22","a23","a24","a25","a26","a27","a28","a29","a30","a31","a32","a33","a34","a35","a36","a37","a38","a39","a40","a41","a42","a43","a44","a45","a46","a47","a48","a49","a50","a51","a52","a53","a54","a55","a56","a57","a58","a59","a60","a61","a62","a63","a64","a65","a66","a67","a68","a69","a70","a71","a72","a73","a74","a75","a76","a77","a78","a79","a80","a81","a82","a83","a84","a85","a86","a87","a88","a89","a90","a91","a92","a93","a94","a95","a96","a97","a98","a99","a100","a101","a102","a103","a104","a105","a106","a107","a108","a109","a110","a111","a112","a113","a114","a115","a116","a117","a118","a119","a120","a121","a122","a123","a124","a125","a126","a127","a128","a129","a130","a131","a132","a133","a134","a135","a136","a137","a138","a139","a140","a141","a142","a143","a144","a145","a146","a147","a148","a149","a150","a151","a152","a153","a154","a155","a156","a157","a158","a159","a160","a161","a162","a163","a164","a165","a166","a167","a168","a169","a170","a171","a172","a173","a174","a175","a176","a177","a178","a179","a180","a181","a182","a183","a184","a185","a186","a187","a188","a189","a190","a191","a192","a193","a194","a195","a196","a197","a198","a199","a200","a201","a202","a203","a204","a205","a206","a207","a208","a209","a210","a211","a212","a213","a214","a215","a216","a217","a218","a219","a220","a221","a222","a223","a224","a225","a226","a227","a228","a229","a230","a231","a232","a233","a234","a235","a236","a237","a238","a239","a240","a241","a242","a243","a244","a245","a246","a247","a248","a249","a250","a251","a252","a253","a254","a255","s6","s7","s100","s101","s90","s91","s92","s93","s94","s95","s96","s97","s98","s99","vcc","scc","memory");
}

extern "C" void kernel_launch(void* const* d_in, const int* in_sizes, int n_in, void* d_out, int out_size, void* d_ws, size_t ws_size, hipStream_t stream) {
  (void)hipFuncSetAttribute((const void*)mk, hipFuncAttributeMaxDynamicSharedMemorySize, LDS_BYTES);
  mk<<<256, 256, LDS_BYTES, stream>>>((const u16*)d_in[0], (const u16*)d_in[1], (const u16*)d_in[2], (const u16*)d_in[4], (const u16*)d_in[5],
                                      (const u16*)d_in[6], (u16*)d_out, (char*)d_ws);
}
```
